```python
import torch

def _softmax(x, axis=-1):
    return torch.softmax(x, dim=axis)
def _gelu(x):
    return torch.nn.functional.gelu(x, approximate="tanh")
def _silu(x):
    return torch.nn.functional.silu(x)
def _sigmoid(x):
    return torch.sigmoid(x)
def _layernorm(x, axis=-1, eps=1e-5):
    m = x.mean(dim=axis, keepdim=True)
    v = x.var(dim=axis, keepdim=True, unbiased=False)
    return (x - m) / torch.sqrt(v + eps)
def _scatter_add(tgt, idx, src, axis=0):
    return tgt.clone().index_add_(axis, idx, src)
def _segment_sum(src, seg, n):
    out = torch.zeros((n,) + src.shape[1:], dtype=src.dtype)
    return out.index_add_(0, seg, src)
def _pad(x, pw):
    flat = tuple(v for pair in reversed(pw) for v in pair)
    return torch.nn.functional.pad(x, flat)

kernel_name = 'sequential_preLN__spectral_fnet__dense_activation_squared_relu__prompt'

def setup_inputs():

    _w = (lambda a: (lambda t: t.to(torch.bfloat16) if t.is_floating_point() else t)(torch.as_tensor(a)))
    def _A_setup():
        B, S, D = 1, 2048, 640
        ns = torch.arange(S)[:, None] * torch.arange(S)[None, :]
        nd = torch.arange(D)[:, None] * torch.arange(D)[None, :]
        Ws_re = torch.cos(-2.0 * torch.pi * ns / S).to(float)
        Ws_im = torch.sin(-2.0 * torch.pi * ns / S).to(float)
        Wd_re = torch.cos(-2.0 * torch.pi * nd / D).to(float)
        Wd_im = torch.sin(-2.0 * torch.pi * nd / D).to(float)
        return {
            "x": _w((lambda *s: torch.randn(*s, dtype=torch.bfloat16) * 0.05)(B, S, D)),
            "dft_seq_re": _w(Ws_re),
            "dft_seq_im": _w(Ws_im),
            "dft_hid_re": _w(Wd_re),
            "dft_hid_im": _w(Wd_im),
        }
    _A_in = _A_setup()

    def _B_setup():
        B, S, D, F = 1, 2048, 640, 1536
        return {
            "x": _w((lambda *s: torch.randn(*s, dtype=torch.bfloat16) * 0.05)(B, S, D)),
            "w1": _w((lambda *s: torch.randn(*s, dtype=torch.bfloat16) * 0.2)(D, F)),
            "b1": _w((lambda *s: torch.randn(*s, dtype=torch.bfloat16) * 0.2)(F)),
            "w2": _w((lambda *s: torch.randn(*s, dtype=torch.bfloat16) * 0.2)(F, D)),
            "b2": _w((lambda *s: torch.randn(*s, dtype=torch.bfloat16) * 0.2)(D)),
        }
    _B_in = _B_setup()

    return {
        "A_x": _A_in["x"],
        "A_dft_seq_re": _A_in["dft_seq_re"],
        "A_dft_seq_im": _A_in["dft_seq_im"],
        "A_dft_hid_re": _A_in["dft_hid_re"],
        "A_dft_hid_im": _A_in["dft_hid_im"],
        "B_x": _B_in["x"],
        "B_w1": _B_in["w1"],
        "B_b1": _B_in["b1"],
        "B_w2": _B_in["w2"],
        "B_b2": _B_in["b2"],
    }

def reference(A_x, A_dft_seq_re, A_dft_seq_im, A_dft_hid_re, A_dft_hid_im, B_x, B_w1, B_b1, B_w2, B_b2):
    def _A_ref(x, dft_seq_re, dft_seq_im, dft_hid_re, dft_hid_im):
        a = torch.einsum("bsd,ed->bse", x, dft_hid_re)
        b = torch.einsum("bsd,ed->bse", x, dft_hid_im)
        y = torch.einsum("ts,bse->bte", dft_seq_re, a) - torch.einsum("ts,bse->bte", dft_seq_im, b)
        return y

    def _B_ref(x, w1, b1, w2, b2):
        h = torch.einsum("bsd,df->bsf", x, w1) + b1
        zero = torch.zeros_like(h)
        r = torch.where(h > zero, h, zero)
        h = r * r
        out = torch.einsum("bsf,fd->bsd", h, w2) + b2
        return out

    x = A_x
    h = x + _A_ref(_layernorm(x, axis=-1), A_dft_seq_re, A_dft_seq_im, A_dft_hid_re, A_dft_hid_im)
    h2 = h + _B_ref(_layernorm(h, axis=-1), B_w1, B_b1, B_w2, B_b2)
    return h2
```

```cpp
#include <hip/hip_runtime.h>
#include <hip/hip_bf16.h>
typedef unsigned short u16;
typedef short bf16x8 __attribute__((ext_vector_type(8)));
typedef float f32x4 __attribute__((ext_vector_type(4)));
typedef unsigned u32x4 __attribute__((ext_vector_type(4)));
constexpr int S = 2048, D = 640, F = 1536;

__device__ __forceinline__ float bf2f(u16 v) { return __uint_as_float(((unsigned)v) << 16); }
__device__ __forceinline__ u16 f2bf(float f) { __hip_bfloat16 b = __float2bfloat16(f); return *(u16*)&b; }

__global__ __launch_bounds__(256) void k_ln(const u16* __restrict__ x, u16* __restrict__ out, int transposed) {
  int row = blockIdx.x * 4 + (threadIdx.x >> 6), lane = threadIdx.x & 63;
  float v[10]; float s = 0;
  for (int i = 0; i < 10; ++i) { v[i] = bf2f(x[row * D + lane + 64 * i]); s += v[i]; }
  for (int o = 32; o; o >>= 1) s += __shfl_xor(s, o);
  float m = s * (1.f / D); float q = 0;
  for (int i = 0; i < 10; ++i) { v[i] -= m; q += v[i] * v[i]; }
  for (int o = 32; o; o >>= 1) q += __shfl_xor(q, o);
  float r = rsqrtf(q * (1.f / D) + 1e-5f);
  for (int i = 0; i < 10; ++i) { int c = lane + 64 * i; u16 o = f2bf(v[i] * r); if (transposed) out[c * S + row] = o; else out[row * D + c] = o; }
}
__global__ __launch_bounds__(256) void k_tr(const u16* __restrict__ in, u16* __restrict__ out, int R, int C) {
  int i = blockIdx.x * 256 + threadIdx.x; if (i >= R * C) return; int c = i / R, r = i % R; out[i] = in[r * C + c];
}
template<int EPI>
__global__ __launch_bounds__(256) void k_gemm(const u16* __restrict__ A0, const u16* __restrict__ A1, int lda, const u16* __restrict__ B0, const u16* __restrict__ B1, int ldb,
                       int K, u16* __restrict__ C, int ldc, const u16* __restrict__ res, const u16* __restrict__ bias) {
  __shared__ __attribute__((aligned(16))) u16 sA[64 * 72], sB[64 * 72];
  int bm = blockIdx.y * 64, bn = blockIdx.x * 64;
  int t = threadIdx.x, lane = t & 63, w = t >> 6, wr = w >> 1, wc = w & 1;
  const u16* A = A0; int arow = bm;
  if (EPI == 0 && bm >= S) { A = A1; arow = bm - S; }
  f32x4 acc[2][2] = {};
  for (int k0 = 0; k0 < K; k0 += 64) {
    const u16* B = B0; int kb = k0;
    if (EPI == 1 && k0 >= D) { B = B1; kb = k0 - D; }
    for (int i = 0; i < 2; ++i) { int idx = t + i * 256; int r = idx >> 3, c = (idx & 7) * 8;
      *(u32x4*)&sA[r * 72 + c] = *(const u32x4*)&A[(size_t)(arow + r) * lda + k0 + c];
      *(u32x4*)&sB[r * 72 + c] = *(const u32x4*)&B[(size_t)(bn + r) * ldb + kb + c]; }
    __syncthreads();
    for (int kk = 0; kk < 2; ++kk) {
      bf16x8 a[2], b[2];
      for (int m = 0; m < 2; ++m) a[m] = *(const bf16x8*)&sA[(wr * 32 + m * 16 + (lane & 15)) * 72 + kk * 32 + (lane >> 4) * 8];
      for (int n = 0; n < 2; ++n) b[n] = *(const bf16x8*)&sB[(wc * 32 + n * 16 + (lane & 15)) * 72 + kk * 32 + (lane >> 4) * 8];
      for (int m = 0; m < 2; ++m) for (int n = 0; n < 2; ++n) acc[m][n] = __builtin_amdgcn_mfma_f32_16x16x32_bf16(a[m], b[n], acc[m][n], 0, 0, 0);
    }
    __syncthreads();
  }
  for (int m = 0; m < 2; ++m) for (int n = 0; n < 2; ++n) for (int j = 0; j < 4; ++j) {
    int r = bm + wr * 32 + m * 16 + (lane >> 4) * 4 + j, c = bn + wc * 32 + n * 16 + (lane & 15);
    float v = acc[m][n][j];
    if (EPI == 0) { if (r >= S) C[(size_t)(r - S) * ldc + D + c] = f2bf(-v); else C[(size_t)r * ldc + c] = f2bf(v); }
    else if (EPI == 1) C[(size_t)r * ldc + c] = f2bf(v + bf2f(res[(size_t)r * D + c]));
    else if (EPI == 2) { v += bf2f(bias[c]); v = v > 0 ? v : 0; C[(size_t)r * ldc + c] = f2bf(v * v); }
    else C[(size_t)r * ldc + c] = f2bf(v + bf2f(bias[c]) + bf2f(res[(size_t)r * D + c]));
  }
}

extern "C" void kernel_launch(void* const* d_in, const int* in_sizes, int n_in, void* d_out, int out_size, void* d_ws, size_t ws_size, hipStream_t stream) {
  const u16* x = (const u16*)d_in[0]; const u16* wsr = (const u16*)d_in[1]; const u16* wsi = (const u16*)d_in[2];
  const u16* wdr = (const u16*)d_in[3]; const u16* wdi = (const u16*)d_in[4];
  const u16* w1 = (const u16*)d_in[6]; const u16* b1 = (const u16*)d_in[7]; const u16* w2 = (const u16*)d_in[8]; const u16* b2 = (const u16*)d_in[9];
  u16* ws = (u16*)d_ws;
  u16* xnT = ws; u16* P = xnT + S * D; u16* h = P + S * 2 * D; u16* hn = h + S * D; u16* g = hn + S * D; u16* w1t = g + S * F; u16* w2t = w1t + D * F;
  k_ln<<<S / 4, 256, 0, stream>>>(x, xnT, 1);
  k_tr<<<D * F / 256, 256, 0, stream>>>(w1, w1t, D, F);
  k_tr<<<D * F / 256, 256, 0, stream>>>(w2, w2t, F, D);
  k_gemm<0><<<dim3(D / 64, 2 * S / 64), 256, 0, stream>>>(wsr, wsi, S, xnT, xnT, S, S, P, 2 * D, nullptr, nullptr);
  k_gemm<1><<<dim3(D / 64, S / 64), 256, 0, stream>>>(P, P, 2 * D, wdr, wdi, D, 2 * D, h, D, x, nullptr);
  k_ln<<<S / 4, 256, 0, stream>>>(h, hn, 0);
  k_gemm<2><<<dim3(F / 64, S / 64), 256, 0, stream>>>(hn, hn, D, w1t, w1t, D, D, g, F, nullptr, b1);
  k_gemm<3><<<dim3(D / 64, S / 64), 256, 0, stream>>>(g, g, F, w2t, w2t, F, F, (u16*)d_out, D, h, b2);
}
```

```cpp
#include <hip/hip_runtime.h>
#include <hip/hip_bf16.h>
typedef unsigned short u16;
typedef short bf16x8 __attribute__((ext_vector_type(8)));
typedef float f32x4 __attribute__((ext_vector_type(4)));
typedef unsigned u32x4 __attribute__((ext_vector_type(4)));
constexpr int S = 2048, D = 640, F = 1536;

__device__ __forceinline__ float bf2f(u16 v) { return __uint_as_float(((unsigned)v) << 16); }
__device__ __forceinline__ u16 f2bf(float f) { __hip_bfloat16 b = __float2bfloat16(f); return *(u16*)&b; }

__global__ __launch_bounds__(256) void k_ln(const u16* __restrict__ x, u16* __restrict__ out, int transposed) {
  int row = blockIdx.x * 4 + (threadIdx.x >> 6), lane = threadIdx.x & 63;
  float v[10]; float s = 0;
  for (int i = 0; i < 10; ++i) { v[i] = bf2f(x[row * D + lane + 64 * i]); s += v[i]; }
  for (int o = 32; o; o >>= 1) s += __shfl_xor(s, o);
  float m = s * (1.f / D); float q = 0;
  for (int i = 0; i < 10; ++i) { v[i] -= m; q += v[i] * v[i]; }
  for (int o = 32; o; o >>= 1) q += __shfl_xor(q, o);
  float r = rsqrtf(q * (1.f / D) + 1e-5f);
  for (int i = 0; i < 10; ++i) { int c = lane + 64 * i; u16 o = f2bf(v[i] * r); if (transposed) out[c * S + row] = o; else out[row * D + c] = o; }
}
__global__ __launch_bounds__(256) void k_tr(const u16* __restrict__ in, u16* __restrict__ out, int R, int C) {
  int i = blockIdx.x * 256 + threadIdx.x; if (i >= R * C) return; int c = i / R, r = i % R; out[i] = in[r * C + c];
}
template<int EPI>
__global__ __launch_bounds__(256) void k_gemm(const u16* __restrict__ A0, const u16* __restrict__ A1, int lda, const u16* __restrict__ B0, const u16* __restrict__ B1, int ldb,
                       int K, u16* __restrict__ C, int ldc, const u16* __restrict__ res, const u16* __restrict__ bias) {
  __shared__ __attribute__((aligned(16))) u16 sA[64 * 72], sB[64 * 72];
  int bm = blockIdx.y * 64, bn = blockIdx.x * 64;
  int t = threadIdx.x, lane = t & 63, w = t >> 6, wr = w >> 1, wc = w & 1;
  const u16* A = A0; int arow = bm;
  if (EPI == 0 && bm >= S) { A = A1; arow = bm - S; }
  f32x4 acc[2][2] = {};
  for (int k0 = 0; k0 < K; k0 += 64) {
    const u16* B = B0; int kb = k0;
    if (EPI == 1 && k0 >= D) { B = B1; kb = k0 - D; }
    for (int i = 0; i < 2; ++i) { int idx = t + i * 256; int r = idx >> 3, c = (idx & 7) * 8;
      *(u32x4*)&sA[r * 72 + c] = *(const u32x4*)&A[(size_t)(arow + r) * lda + k0 + c];
      *(u32x4*)&sB[r * 72 + c] = *(const u32x4*)&B[(size_t)(bn + r) * ldb + kb + c]; }
    __syncthreads();
    for (int kk = 0; kk < 2; ++kk) {
      bf16x8 a[2], b[2];
      for (int m = 0; m < 2; ++m) a[m] = *(const bf16x8*)&sA[(wr * 32 + m * 16 + (lane & 15)) * 72 + kk * 32 + (lane >> 4) * 8];
      for (int n = 0; n < 2; ++n) b[n] = *(const bf16x8*)&sB[(wc * 32 + n * 16 + (lane & 15)) * 72 + kk * 32 + (lane >> 4) * 8];
      for (int m = 0; m < 2; ++m) for (int n = 0; n < 2; ++n) acc[m][n] = __builtin_amdgcn_mfma_f32_16x16x32_bf16(a[m], b[n], acc[m][n], 0, 0, 0);
    }
    __syncthreads();
  }
  for (int m = 0; m < 2; ++m) for (int n = 0; n < 2; ++n) for (int j = 0; j < 4; ++j) {
    int r = bm + wr * 32 + m * 16 + (lane >> 4) * 4 + j, c = bn + wc * 32 + n * 16 + (lane & 15);
    float v = acc[m][n][j];
    if (EPI == 0) { if (r >= S) C[(size_t)(r - S) * ldc + D + c] = f2bf(-v); else C[(size_t)r * ldc + c] = f2bf(v); }
    else if (EPI == 1) C[(size_t)r * ldc + c] = f2bf(v + bf2f(res[(size_t)r * D + c]));
    else if (EPI == 2) { v += bf2f(bias[c]); v = v > 0 ? v : 0; C[(size_t)r * ldc + c] = f2bf(v * v); }
    else C[(size_t)r * ldc + c] = f2bf(v + bf2f(bias[c]) + bf2f(res[(size_t)r * D + c]));
  }
}


__global__ __launch_bounds__(512) void k_probe(const u32x4* __restrict__ a, const u32x4* __restrict__ b, unsigned* sink) {
  u32x4 v[8];
  size_t base = (size_t)blockIdx.x * 2048 + threadIdx.x;
  for (int i = 0; i < 4; ++i) { v[i] = a[base + i * 512]; v[4 + i] = b[base + i * 512]; }
  u32x4 acc = v[0];
  for (int i = 1; i < 8; ++i) acc ^= v[i];
  if (acc.x == 0x12345678 && acc.y == 0x9abcdef0 && acc.z == 77) sink[0] = acc.w;
}
extern "C" void kernel_launch(void* const* d_in, const int* in_sizes, int n_in, void* d_out, int out_size, void* d_ws, size_t ws_size, hipStream_t stream) {
  const u16* x = (const u16*)d_in[0]; const u16* wsr = (const u16*)d_in[1]; const u16* wsi = (const u16*)d_in[2];
  const u16* wdr = (const u16*)d_in[3]; const u16* wdi = (const u16*)d_in[4];
  const u16* w1 = (const u16*)d_in[6]; const u16* b1 = (const u16*)d_in[7]; const u16* w2 = (const u16*)d_in[8]; const u16* b2 = (const u16*)d_in[9];
  u16* ws = (u16*)d_ws;
  u16* xnT = ws; u16* P = xnT + S * D; u16* h = P + S * 2 * D; u16* hn = h + S * D; u16* g = hn + S * D; u16* w1t = g + S * F; u16* w2t = w1t + D * F;
  k_probe<<<256, 512, 0, stream>>>((const u32x4*)wsr, (const u32x4*)wsi, (unsigned*)(w2t + D * F));
  k_ln<<<S / 4, 256, 0, stream>>>(x, xnT, 1);
  k_tr<<<D * F / 256, 256, 0, stream>>>(w1, w1t, D, F);
  k_tr<<<D * F / 256, 256, 0, stream>>>(w2, w2t, F, D);
  k_gemm<0><<<dim3(D / 64, 2 * S / 64), 256, 0, stream>>>(wsr, wsi, S, xnT, xnT, S, S, P, 2 * D, nullptr, nullptr);
  k_gemm<1><<<dim3(D / 64, S / 64), 256, 0, stream>>>(P, P, 2 * D, wdr, wdi, D, 2 * D, h, D, x, nullptr);
  k_ln<<<S / 4, 256, 0, stream>>>(h, hn, 0);
  k_gemm<2><<<dim3(F / 64, S / 64), 256, 0, stream>>>(hn, hn, D, w1t, w1t, D, D, g, F, nullptr, b1);
  k_gemm<3><<<dim3(D / 64, S / 64), 256, 0, stream>>>(g, g, F, w2t, w2t, F, F, (u16*)d_out, D, h, b2);
}
```

```cpp
#include <hip/hip_runtime.h>
#include <hip/hip_bf16.h>
typedef unsigned short u16;
typedef short bf16x8 __attribute__((ext_vector_type(8)));
typedef float f32x4 __attribute__((ext_vector_type(4)));
typedef unsigned u32x4 __attribute__((ext_vector_type(4)));
typedef __attribute__((address_space(1))) unsigned gu32;
#define RLX __ATOMIC_RELAXED, __HIP_MEMORY_SCOPE_AGENT
constexpr int S = 2048, D = 640, F = 1536;
constexpr int NB = 256;

__device__ __forceinline__ float bf2f(u16 v) { return __uint_as_float(((unsigned)v) << 16); }
__device__ __forceinline__ u16 f2bf(float f) { __hip_bfloat16 b = __float2bfloat16(f); return *(u16*)&b; }

__device__ void d_ln(const u16* x, u16* out, int transposed, int blk) {
  int row = blk * 4 + (threadIdx.x >> 6), lane = threadIdx.x & 63;
  float v[10]; float s = 0;
  for (int i = 0; i < 10; ++i) { v[i] = bf2f(x[row * D + lane + 64 * i]); s += v[i]; }
  for (int o = 32; o; o >>= 1) s += __shfl_xor(s, o);
  float m = s * (1.f / D); float q = 0;
  for (int i = 0; i < 10; ++i) { v[i] -= m; q += v[i] * v[i]; }
  for (int o = 32; o; o >>= 1) q += __shfl_xor(q, o);
  float r = rsqrtf(q * (1.f / D) + 1e-5f);
  for (int i = 0; i < 10; ++i) { int c = lane + 64 * i; u16 o = f2bf(v[i] * r); if (transposed) out[c * S + row] = o; else out[row * D + c] = o; }
}
__device__ void d_tr(const u16* in, u16* out, int R, int C, int blk) {
  int i = blk * 256 + threadIdx.x; int c = i / R, r = i % R; out[i] = in[r * C + c];
}
template<int EPI>
__device__ void d_gemm(u16* sA, u16* sB, int bx, int by, const u16* A0, const u16* A1, int lda, const u16* B0, const u16* B1, int ldb,
                       int K, u16* C, int ldc, const u16* res, const u16* bias) {
  int bm = by * 64, bn = bx * 64;
  int t = threadIdx.x, lane = t & 63, w = t >> 6, wr = w >> 1, wc = w & 1;
  const u16* A = A0; int arow = bm;
  if (EPI == 0 && bm >= S) { A = A1; arow = bm - S; }
  f32x4 acc[2][2] = {};
  for (int k0 = 0; k0 < K; k0 += 64) {
    const u16* B = B0; int kb = k0;
    if (EPI == 1 && k0 >= D) { B = B1; kb = k0 - D; }
    for (int i = 0; i < 2; ++i) { int idx = t + i * 256; int r = idx >> 3, c = (idx & 7) * 8;
      *(u32x4*)&sA[r * 72 + c] = *(const u32x4*)&A[(size_t)(arow + r) * lda + k0 + c];
      *(u32x4*)&sB[r * 72 + c] = *(const u32x4*)&B[(size_t)(bn + r) * ldb + kb + c]; }
    __syncthreads();
    for (int kk = 0; kk < 2; ++kk) {
      bf16x8 a[2], b[2];
      for (int m = 0; m < 2; ++m) a[m] = *(const bf16x8*)&sA[(wr * 32 + m * 16 + (lane & 15)) * 72 + kk * 32 + (lane >> 4) * 8];
      for (int n = 0; n < 2; ++n) b[n] = *(const bf16x8*)&sB[(wc * 32 + n * 16 + (lane & 15)) * 72 + kk * 32 + (lane >> 4) * 8];
      for (int m = 0; m < 2; ++m) for (int n = 0; n < 2; ++n) acc[m][n] = __builtin_amdgcn_mfma_f32_16x16x32_bf16(a[m], b[n], acc[m][n], 0, 0, 0);
    }
    __syncthreads();
  }
  for (int m = 0; m < 2; ++m) for (int n = 0; n < 2; ++n) for (int j = 0; j < 4; ++j) {
    int r = bm + wr * 32 + m * 16 + (lane >> 4) * 4 + j, c = bn + wc * 32 + n * 16 + (lane & 15);
    float v = acc[m][n][j];
    if (EPI == 0) { if (r >= S) C[(size_t)(r - S) * ldc + D + c] = f2bf(-v); else C[(size_t)r * ldc + c] = f2bf(v); }
    else if (EPI == 1) C[(size_t)r * ldc + c] = f2bf(v + bf2f(res[(size_t)r * D + c]));
    else if (EPI == 2) { v += bf2f(bias[c]); v = v > 0 ? v : 0; C[(size_t)r * ldc + c] = f2bf(v * v); }
    else C[(size_t)r * ldc + c] = f2bf(v + bf2f(bias[c]) + bf2f(res[(size_t)r * D + c]));
  }
}
__device__ void gbar(gu32* flags, int phase, unsigned ep) {
  asm volatile("s_waitcnt vmcnt(0)" ::: "memory");
  __syncthreads();
  if (threadIdx.x < 64) {
    gu32* f = flags + phase * NB;
    if (threadIdx.x == 0) { __builtin_amdgcn_fence(__ATOMIC_RELEASE, "agent"); asm volatile("s_waitcnt vmcnt(0)" ::: "memory"); __hip_atomic_store(f + blockIdx.x, ep, RLX); }
    for (unsigned sp = 0; sp < (1u << 20); ++sp) {
      bool ok = true;
      for (int i = 0; i < 4; ++i) ok &= __hip_atomic_load(f + threadIdx.x * 4 + i, RLX) == ep;
      if (__all(ok)) break;
      __builtin_amdgcn_s_sleep(2);
    }
    __builtin_amdgcn_fence(__ATOMIC_ACQUIRE, "agent");
    asm volatile("s_waitcnt vmcnt(0)" ::: "memory");
  }
  __syncthreads();
}
__global__ __launch_bounds__(256, 1) void k_all(const u16* x, const u16* wsr, const u16* wsi, const u16* wdr, const u16* wdi, const u16* w1, const u16* b1, const u16* w2, const u16* b2, u16* out, u16* ws) {
  __shared__ __attribute__((aligned(16))) u16 sA[64 * 72], sB[64 * 72];
  gu32* ctl = (gu32*)ws;
  gu32* flags = ctl + 64;
  u16* xnT = ws + 65536; u16* P = xnT + S * D; u16* h = P + S * 2 * D; u16* hn = h + S * D; u16* g = hn + S * D; u16* w1t = g + S * F; u16* w2t = w1t + D * F;
  unsigned ep = __hip_atomic_load(ctl, RLX) + 1;
  int b = blockIdx.x;
  for (int i = b; i < S / 4; i += NB) d_ln(x, xnT, 1, i);
  for (int i = b; i < D * F / 256; i += NB) { d_tr(w1, w1t, D, F, i); d_tr(w2, w2t, F, D, i); }
  gbar(flags, 0, ep);
  if (b == 0 && threadIdx.x == 0) __hip_atomic_store(ctl, ep, RLX);
  for (int i = b; i < 10 * 64; i += NB) d_gemm<0>(sA, sB, i % 10, i / 10, wsr, wsi, S, xnT, xnT, S, S, P, 2 * D, nullptr, nullptr);
  gbar(flags, 1, ep);
  for (int i = b; i < 10 * 32; i += NB) d_gemm<1>(sA, sB, i % 10, i / 10, P, P, 2 * D, wdr, wdi, D, 2 * D, h, D, x, nullptr);
  gbar(flags, 2, ep);
  for (int i = b; i < S / 4; i += NB) d_ln(h, hn, 0, i);
  gbar(flags, 3, ep);
  for (int i = b; i < 24 * 32; i += NB) d_gemm<2>(sA, sB, i % 24, i / 24, hn, hn, D, w1t, w1t, D, D, g, F, nullptr, b1);
  gbar(flags, 4, ep);
  for (int i = b; i < 10 * 32; i += NB) d_gemm<3>(sA, sB, i % 10, i / 10, g, g, F, w2t, w2t, F, F, out, D, h, b2);
}

extern "C" void kernel_launch(void* const* d_in, const int* in_sizes, int n_in, void* d_out, int out_size, void* d_ws, size_t ws_size, hipStream_t stream) {
  k_all<<<NB, 256, 0, stream>>>((const u16*)d_in[0], (const u16*)d_in[1], (const u16*)d_in[2], (const u16*)d_in[3], (const u16*)d_in[4],
     (const u16*)d_in[6], (const u16*)d_in[7], (const u16*)d_in[8], (const u16*)d_in[9], (u16*)d_out, (u16*)d_ws);
}
```

```cpp
#include <hip/hip_runtime.h>

typedef unsigned short u16;
typedef unsigned long long u64;
typedef short bf16x8 __attribute__((ext_vector_type(8)));
typedef short s16x4 __attribute__((ext_vector_type(4)));
typedef __bf16 bf16x2v __attribute__((ext_vector_type(2)));
typedef float f32x4 __attribute__((ext_vector_type(4)));
typedef float f32x2 __attribute__((ext_vector_type(2)));
typedef unsigned u32x4 __attribute__((ext_vector_type(4)));
typedef unsigned u32x2 __attribute__((ext_vector_type(2)));
typedef __amdgpu_buffer_rsrc_t rsrc_t;
typedef __attribute__((address_space(1))) u64 gu64;
#define LAS __attribute__((address_space(3)))
#define DI __device__ __forceinline__
#define RLX_AGENT __ATOMIC_RELAXED, __HIP_MEMORY_SCOPE_AGENT

constexpr int S = 2048, D = 640, F = 1536;
constexpr unsigned WS_CTL = 0;
constexpr unsigned WS_FAB = 32768;
constexpr unsigned WS_F1 = 262144;
constexpr unsigned WS_ST = 294912;
constexpr unsigned WS_CS = 327680;
constexpr unsigned WS_FH = 393216;
constexpr unsigned WS_FG = 524288;
constexpr unsigned WS_FP = 1048576;
constexpr unsigned WS_AB8 = 1310720;
constexpr unsigned WS_W18 = WS_AB8 + 2 * S * D * 2;
constexpr unsigned WS_W28 = WS_W18 + D * F * 2;
constexpr unsigned WS_PART = WS_W28 + D * F * 2;
constexpr unsigned WS_H8 = WS_PART + 4 * S * D * 2;
constexpr unsigned WS_HST = WS_H8 + S * D * 2;
constexpr unsigned WS_G8 = WS_HST + 32 * 8 * 64 * 8;
constexpr unsigned WS_END = WS_G8 + S * F * 2;
constexpr unsigned L_A = 0, L_B = 4 * 16384, L_R = 16384, L_T = 147456, L_END = L_T + 16384;

DI rsrc_t mk_rsrc(const void* p, unsigned bytes) { return __builtin_amdgcn_make_buffer_rsrc((void*)p, 0, bytes, 0x00020000); }
DI unsigned pk_bf16(float lo, float hi) { f32x2 f = {lo, hi}; return __builtin_bit_cast(unsigned, __builtin_convertvector(f, bf16x2v)); }
DI float bf_lo(unsigned u) { return __uint_as_float(u << 16); }
DI float bf_hi(unsigned u) { return __uint_as_float(u & 0xffff0000u); }
template <int CTRL> DI float dpp_add(float v) { return v + __int_as_float(__builtin_amdgcn_update_dpp(0, __float_as_int(v), CTRL, 0xf, 0xf, true)); }
DI float sum16(float v) { v = dpp_add<0xB1>(v); v = dpp_add<0x4E>(v); v = dpp_add<0x141>(v); return dpp_add<0x140>(v); }
DI float xadd16(float v) { auto r = __builtin_amdgcn_permlane16_swap(__float_as_uint(v), __float_as_uint(v), false, false); return __uint_as_float(r[0]) + __uint_as_float(r[1]); }
DI float xadd32(float v) { auto r = __builtin_amdgcn_permlane32_swap(__float_as_uint(v), __float_as_uint(v), false, false); return __uint_as_float(r[0]) + __uint_as_float(r[1]); }
DI void mfma_vv(f32x4& c, const bf16x8& a, const bf16x8& b) { asm volatile("v_mfma_f32_16x16x32_bf16 %0, %1, %2, %0" : "+v"(c) : "v"(a), "v"(b) : "memory"); }
DI void mfma_av(f32x4& c, const bf16x8& a, const bf16x8& b) { asm volatile("v_mfma_f32_16x16x32_bf16 %0, %1, %2, %0" : "+v"(c) : "a"(a), "v"(b) : "memory"); }
DI void mfma_av0(f32x4& c, const bf16x8& a, const bf16x8& b) { asm volatile("v_mfma_f32_16x16x32_bf16 %0, %1, %2, 0" : "=&v"(c) : "a"(a), "v"(b) : "memory"); }
DI void mfma_vv0(f32x4& c, const bf16x8& a, const bf16x8& b) { asm volatile("v_mfma_f32_16x16x32_bf16 %0, %1, %2, 0" : "=&v"(c) : "v"(a), "v"(b) : "memory"); }
template <bool AG, bool FIRST> DI void mfma_w(f32x4& c, const bf16x8& a, const bf16x8& b) {
  if constexpr (AG && FIRST) mfma_av0(c, a, b); else if constexpr (AG) mfma_av(c, a, b); else if constexpr (FIRST) mfma_vv0(c, a, b); else mfma_vv(c, a, b);
}
#define MFMA_DRAIN() asm volatile("s_nop 7\n\ts_nop 1" ::: "memory")
DI void pin(f32x4& c) { asm volatile("" : "+v"(c)); }
#define WAIT_VM(n) asm volatile("s_waitcnt vmcnt(" #n ")" ::: "memory")
#define BARRIER() do { asm volatile("s_waitcnt lgkmcnt(0)" ::: "memory"); __builtin_amdgcn_s_barrier(); asm volatile("" ::: "memory"); } while (0)
template <int N> DI void wait_vm() { asm volatile("s_waitcnt vmcnt(%0)" :: "i"(N) : "memory"); }

DI void set_m0(unsigned v) { asm volatile("s_mov_b32 m0, %0\n\ts_nop 0" :: "s"(v) : "memory"); }
template <int IMM> DI void set_m0n(unsigned base) { asm volatile("s_add_u32 m0, %0, %1\n\ts_nop 0" :: "s"(base), "i"(IMM) : "memory", "scc"); }
template <int OFF, int POL> DI void dma(unsigned voff, rsrc_t r, unsigned soff) {
  if constexpr (POL == 0) asm volatile("buffer_load_dwordx4 %0, %1, %2 offen offset:%3 lds" :: "v"(voff), "s"(r), "s"(soff), "i"(OFF) : "memory");
  if constexpr (POL == 2) asm volatile("buffer_load_dwordx4 %0, %1, %2 offen offset:%3 sc1 lds" :: "v"(voff), "s"(r), "s"(soff), "i"(OFF) : "memory");
}
template <int OFF> DI void lds_rd(bf16x8& d, unsigned a) { asm volatile("ds_read_b128 %0, %1 offset:%2" : "=v"(d) : "v"(a), "i"(OFF) : "memory"); }
template <int N> DI void wait_lgkm() { asm volatile("s_waitcnt lgkmcnt(%0)" :: "i"(N) : "memory"); }
template <int N> DI void wait_vm_lgkm0() { asm volatile("s_waitcnt vmcnt(%0) lgkmcnt(0)" :: "i"(N) : "memory"); }
DI bf16x8 ld_sc1(rsrc_t r, unsigned voff, unsigned soff) { return __builtin_bit_cast(bf16x8, __builtin_amdgcn_raw_buffer_load_b128(r, voff, soff, 16)); }
DI void st_sc1(u32x4 v, rsrc_t r, unsigned voff, unsigned soff) { __builtin_amdgcn_raw_buffer_store_b128(v, r, voff + soff, 0, 16); }
DI bf16x8 lds_ld(unsigned a) { return *(LAS bf16x8*)a; }
DI s16x4 lds_tr(unsigned a) { return __builtin_amdgcn_ds_read_tr16_b64_v4i16((LAS s16x4*)a); }

template <int V> struct IC { static constexpr int value = V; };
constexpr unsigned SPIN_MAX = 1u << 20;
DI bool flag_missing(u32x2 f, u64 ep) { return __builtin_amdgcn_ballot_w64(__builtin_bit_cast(u64, f) != ep) != 0; }
DI void poll(rsrc_t rws, unsigned off, unsigned voff, u64 ep) {
#pragma unroll 1
  for (unsigned it = 0;;) {
    asm volatile("" : "+s"(it) :: "memory");
    const u32x2 a = __builtin_amdgcn_raw_buffer_load_b64(rws, voff, off, 16);
    if (!flag_missing(a, ep)) break;
    asm volatile("" ::: "memory");
    if (++it == SPIN_MAX) break;
  }
  asm volatile("" ::: "memory");
}
DI unsigned flag_off(int lane, bool on, unsigned off) { return on && lane < 8 ? off + lane * 16 : 0xF0000000u; }
DI void put_flag(rsrc_t rws, unsigned voff, u64 ep) {
  __builtin_amdgcn_raw_buffer_store_b128((u32x4){(unsigned)ep, (unsigned)(ep >> 32), (unsigned)ep, (unsigned)(ep >> 32)}, rws, voff, 0, 16);
  asm volatile("" ::: "memory");
}
DI void set_flag(char* ws, unsigned off, u64 ep) { __hip_atomic_store((gu64*)(ws + off), ep, RLX_AGENT); }

DI void ld64_sc1_uncounted(u32x2& d, rsrc_t r, unsigned voff, unsigned soff) { asm volatile("buffer_load_dwordx2 %0, %1, %2, %3 offen sc1" : "=v"(d) : "v"(voff), "s"(r), "s"(soff) : "memory"); }
DI void v_fma(float& d, float a, float b, float c) { asm volatile("v_fma_f32 %0, %1, %2, %3" : "=v"(d) : "v"(a), "v"(b), "v"(c)); }
DI void v_nfma(float& d, float a, float b, float c) { asm volatile("v_fma_f32 %0, -%1, %2, %3" : "=v"(d) : "v"(a), "v"(b), "v"(c)); }
DI void v_relu(float& d) { asm volatile("v_max_f32 %0, 0, %0" : "+v"(d)); }
DI void v_sqr(float& d) { asm volatile("v_mul_f32 %0, %0, %0" : "+v"(d)); }
DI void v_mul(float& d, float a) { asm volatile("v_mul_f32 %0, %0, %1" : "+v"(d) : "v"(a)); }
DI void v_add(float& d, float a) { asm volatile("v_add_f32 %0, %0, %1" : "+v"(d) : "v"(a)); }
DI void v_cvt(unsigned& d, float a, float b) { asm volatile("v_cvt_pk_bf16_f32 %0, %1, %2" : "=v"(d) : "v"(a), "v"(b)); }
DI void v_swap16(unsigned& a, unsigned& b) { asm volatile("v_permlane16_swap_b32 %0, %1" : "+v"(a), "+v"(b)); }
template <int OFF> DI void lds_rd_a(bf16x8& d, unsigned a) { asm volatile("ds_read_b128 %0, %1 offset:%2" : "=a"(d) : "v"(a), "i"(OFF) : "memory"); }
template <int OFF> DI void lds_rd_f4(f32x4& d, unsigned a) { asm volatile("ds_read_b128 %0, %1 offset:%2" : "=v"(d) : "v"(a), "i"(OFF) : "memory"); }
template <int OFF> DI void lds_rd_f2(f32x2& d, unsigned a) { asm volatile("ds_read_b64 %0, %1 offset:%2" : "=v"(d) : "v"(a), "i"(OFF) : "memory"); }
template <int OFF> DI void lds_wr_f4(unsigned a, const f32x4& d) { asm volatile("ds_write_b128 %0, %1 offset:%2" :: "v"(a), "v"(d), "i"(OFF) : "memory"); }
template <int OFF> DI void lds_wr_u4(unsigned a, const u32x4& d, u64 m) { asm volatile("s_mov_b64 exec, %3\n\tds_write_b128 %0, %1 offset:%2\n\ts_mov_b64 exec, -1" :: "v"(a), "v"(d), "i"(OFF), "s"(m) : "memory"); }
template <int OFF> DI void lds_wr_u2(unsigned a, const u32x2& d, u64 m) { asm volatile("s_mov_b64 exec, %3\n\tds_write_b64 %0, %1 offset:%2\n\ts_mov_b64 exec, -1" :: "v"(a), "v"(d), "i"(OFF), "s"(m) : "memory"); }
DI void v_add2(float& d, float a, float b) { asm volatile("v_add_f32 %0, %1, %2" : "=v"(d) : "v"(a), "v"(b)); }
template <bool WA, bool XA, bool FIRST> DI void mfma_x(f32x4& c, const bf16x8& a, const bf16x8& b) {
  if constexpr (!XA) mfma_w<WA, FIRST>(c, a, b);
  else if constexpr (WA && FIRST) asm volatile("v_mfma_f32_16x16x32_bf16 %0, %1, %2, 0" : "=&v"(c) : "a"(a), "a"(b) : "memory");
  else if constexpr (WA) asm volatile("v_mfma_f32_16x16x32_bf16 %0, %1, %2, %0" : "+v"(c) : "a"(a), "a"(b) : "memory");
  else if constexpr (FIRST) asm volatile("v_mfma_f32_16x16x32_bf16 %0, %1, %2, 0" : "=&v"(c) : "v"(a), "a"(b) : "memory");
  else asm volatile("v_mfma_f32_16x16x32_bf16 %0, %1, %2, %0" : "+v"(c) : "v"(a), "a"(b) : "memory");
}
DI void ld_sc1_a(bf16x8& d, rsrc_t r, unsigned voff, unsigned soff) { asm volatile("buffer_load_dwordx4 %0, %1, %2, %3 offen sc1" : "+a"(d) : "v"(voff), "s"(r), "s"(soff) : "memory"); }
DI void undef_a(bf16x8& d) { asm volatile("" : "=a"(d)); }
template <int OFF> DI void ld_uncounted(f32x4& d, rsrc_t r, unsigned voff, unsigned soff) { asm volatile("buffer_load_dwordx4 %0, %1, %2, %3 offen offset:%4" : "=v"(d) : "v"(voff), "s"(r), "s"(soff), "i"(OFF) : "memory"); }
DI void pin2(u32x2& c) { asm volatile("" : "+v"(c)); }
template <int I, int N, class Fn> DI void static_for(Fn&& f) { if constexpr (I < N) { f(IC<I>{}); static_for<I + 1, N>(f); } }

constexpr int F1S = 11;
constexpr int F1L = 18;
constexpr int HXT = 12;
constexpr int H_PW = 13;
constexpr int NAFT = 4;
constexpr int H_WS0 = 14;
constexpr int H_RS = 2;
constexpr int OPOL = 16;
constexpr int H_ST = 7;
constexpr int G1_PRO = 22;
constexpr int G1_LAST = 12;
constexpr int NPRE = 9;
constexpr int PRO2 = 2;
constexpr int TAIL2 = 26;
constexpr int H_FLA = 38;
constexpr int G2_SLOPE = 208;
constexpr int N3_TAIL = 20;
constexpr int N3_PP = 4;
constexpr int N3_HP = 6;
constexpr int N3_R = 12;
constexpr int NB_PRE = 22;
constexpr int N4_L = 19;
constexpr int ACTE = 14;
constexpr int FLEAD = 40;
constexpr int AGS = 48;
constexpr int HCHK = 52;
constexpr int NL_PRE = 10;
constexpr int W4END = 170;
constexpr int UNP0 = 196;
constexpr int NV4 = 24;
constexpr int G4CHK = 24;
constexpr int FLAGD = 32;

enum { K_A, K_B, K_WS, K_ST, K_RS, K_XT, K_F1, K_W3, K_PUT, K_FLAG, K_PW };
struct Ins { int kind, idx, sub, half; };
struct Prog {
  Ins v[224]; int n;
  constexpr void add(int kind, int idx, int s0, int s1, int half) { for (int s = s0; s < s1; ++s) v[n++] = Ins{kind, idx, s, half}; }
  constexpr int lo(int h) const { int i = 0; while (i < n && v[i].half < h) ++i; return i; }
  constexpr int last(int kind, int idx) const { int l = -1; for (int i = 0; i < n; ++i) if (v[i].kind == kind && v[i].idx == idx) l = i; return l; }
};
constexpr int imax(int a, int b) { return a > b ? a : b; }
constexpr int imin(int a, int b) { return a < b ? a : b; }
constexpr Prog g1_prog() {
  Prog p{};
  for (int h = -1; h < 20; ++h) {
    if (h == H_PW) p.add(K_PW, 0, 0, 1, h);
    if (h == H_ST) p.add(K_ST, 0, 0, 2, h);
    int prev = -1;
    for (int i = 0; i < 65; ++i) {
      const int T = i / 13, r = i % 13, kind = r < 5 ? K_B : K_A, idx = r < 5 ? T : 2 * T + (r - 5) / 4, sub = r < 5 ? r : (r - 5) % 4;
      int hh = i < G1_PRO ? -1 : (i - G1_PRO) * (G1_LAST + 1) / (65 - G1_PRO);
      hh = imax(imax(hh, prev), kind == K_A ? (idx >= 4 ? 2 * (idx - 4) + 1 : -1) : (idx >= 3 ? 4 * (idx - 3) + 3 : -1));
      prev = hh;
      if (hh == h) p.add(kind, idx, sub, sub + 1, h);
    }
    if (h == H_WS0 || h == H_WS0 + 1) p.add(K_WS, 0, 2 * (h - H_WS0), 2 * (h - H_WS0) + 2, h);
  }
  return p;
}
constexpr Prog G1P = g1_prog();
constexpr int NPRE0 = NPRE;
constexpr int g1_wait(int h) {
  const int t1 = (h + 1) / 2;
  int l = imax(G1P.last(K_A, t1), G1P.last(K_B, t1 / 2));
  if (h == 13) l = imax(l, G1P.last(K_ST, 0));
  if (h == 15) l = imax(l, G1P.last(K_A, 9));
  return G1P.lo(h) - 1 - l + (l < G1P.lo(0) ? 2 : 0);
}
constexpr int a2slot(int s) { return s == 0 ? 126976 : s == 1 ? 32768 : s == 2 ? 49152 : 0; }
constexpr int b2slot(int s) { return s == 3 ? 143360 : 65536 + s * 20480; }
constexpr int H_XT = HXT, F1_SET = F1S, F1_LOAD = F1L, F1_CHK = 23;
constexpr int HA = 24, HE = 40;
constexpr Prog g2_prog() {
  Prog p{};
  for (int t = 0; t < PRO2; ++t) p.add(K_B, t, 0, 5, -1);
  for (int h = 0; h < HE; ++h) {
    if (h == 0) for (int t = PRO2; t < 4; ++t) p.add(K_B, t, 0, 5, h);
    for (int i = 0; i < 108; ++i) {
      const int t = 4 + i / 9, s = i % 9;
      if ((i < 5 ? 1 + i * 2 / 5 : (100 + i * G2_SLOPE / 9) / 100) == h) { if (s < 5) p.add(K_B, t, s, s + 1, h); else if (t > 4) p.add(K_A, t, s - 5, s - 4, h); }
    }
    if (h == H_RS) p.add(K_RS, 0, 0, 4, h);
    if (h == H_XT) p.add(K_XT, 0, 0, 3, h);
    if (h == F1_LOAD) p.add(K_F1, 0, 0, 1, h);
    if (h == 32 || h == 33) p.add(K_PUT, 0, 3 * (h - 32), 3 * (h - 31), h);
    if (h == H_FLA) p.add(K_FLAG, 0, 0, 1, h);
    for (int i = 0; i < N3_TAIL; ++i) if (h == TAIL2 + i * (HE - TAIL2) / N3_TAIL) p.add(K_W3, 0, i, i + 1, h);
  }
  return p;
}
constexpr Prog G2P = g2_prog();
constexpr int g2_wait(int h) {
  const int t1 = (h + 1) / 2;
  int l = imax(G2P.last(K_A, t1), G2P.last(K_B, t1));
  if (h == F1_SET) l = imax(l, G2P.last(K_RS, 0));
  if (h == F1_CHK) l = imax(l, G2P.last(K_F1, 0));
  return G2P.lo(h) - 1 - l;
}
constexpr bool g2_ok() {
  for (int i = 0; i < G2P.n; ++i) { const Ins e = G2P.v[i]; if ((e.kind == K_A || e.kind == K_B) && e.idx >= 4 && (e.half < 2 * (e.idx - 4) + 1 || e.half >= 2 * e.idx - 1)) return false; }
  for (int h = 1; h < 30; h += 2) if (g2_wait(h) < 0 || g2_wait(h) > 63) return false;
  return true;
}
static_assert(g2_ok());
constexpr int m0_of(const Ins& e) {
  return e.kind == K_A ? 1000 + e.idx : e.kind == K_B ? 2000 + e.idx * 2 + (e.sub >= 3) : e.kind == K_XT ? 3001 : e.kind == K_ST ? 3002 : e.kind == K_WS ? 3003 : -1;
}
constexpr bool setm0(const Prog& P, int i) {
  if (m0_of(P.v[i]) < 0) return false;
  for (int k = i - 1; k >= 0; --k) if (m0_of(P.v[k]) >= 0) return m0_of(P.v[k]) != m0_of(P.v[i]);
  return true;
}
constexpr bool g1_ok() { if (G1P.lo(16) != G1P.n) return false; for (int h = 1; h < 16; h += 2) if (g1_wait(h) < 0 || g1_wait(h) > 63) return false; return true; }
static_assert(g1_ok());

constexpr int NR4 = NV4 + 4, NL4 = N4_L, NF4 = 10, ND4 = 60 - NR4 - NL4 - NF4;
constexpr int NE4 = NR4 + NL4 - NL_PRE;
enum { E_HD, E_W4L, E_W4R, E_W4F, E_W4D, E_ACT, E_GFLAG, E_FLD, E_AG, E_OUT,
       E_AF, E_SRD, E_UNP, E_RW, E_RR,
       E_BARH, E_BARR, E_SADD, E_SFIN, E_KK, E_CHK, E_INIT, E_RADD, E_WL };
struct Ev { short kind, a, b, gap; };
struct Evs {
  Ev v[900]; int n; short first[482];
  constexpr void add(int kind, int a, int b, int gap) { v[n++] = Ev{(short)kind, (short)a, (short)b, (short)gap}; }
  static constexpr bool isvm(const Ev& e) { return e.kind <= E_OUT && (e.kind != E_ACT || e.b == 14 || e.b == 22); }
  static constexpr bool islds(const Ev& e) { return e.kind >= E_AF && e.kind <= E_RR; }
  constexpr int last(int kind, int a, int b, int below) const { int l = -1; for (int i = 0; i < below; ++i) if (v[i].kind == kind && v[i].a == a && (b < 0 || v[i].b == b)) l = i; return l; }
  constexpr int vm(int i0, int i1) const { int c = 0; for (int i = i0 + 1; i < i1; ++i) c += isvm(v[i]); return c < 63 ? c : 63; }
  constexpr int lds(int i0, int i1) const { int c = 0; for (int i = i0 + 1; i < i1; ++i) c += islds(v[i]); return c < 15 ? c : 15; }
};
constexpr Evs ffn_prog() {
  Evs p{};
  for (int G = -1; G < 480; ++G) {
    p.first[G + 1] = (short)p.n;
    if (G < 0) {
      p.add(E_BARH, 0, 0, G);
      for (int s = 0; s < 4; ++s) p.add(E_AF, s, 0, G);
      p.add(E_WL, E_AF, 1, G);
      continue;
    }
    const int B = G / 60, g = G % 60;
    if (B < 4) {
      const int m = B, S = G / 3;
      if (G == imax(HCHK - 40, 0)) p.add(E_FLD, 4, 0, G);
      if (G == HCHK) p.add(E_CHK, 4, 0, G);
      for (int u = 0; u < 11; ++u) if (G == HCHK + 2 * u) p.add(E_HD, u < 6 ? 2 : 3, u < 6 ? u : u - 6, G);
      if (g == 46 && m < 3) p.add(E_BARH, m + 1, 0, G);
      if (g % 6 == 0 && S + 4 < 80) p.add(E_AF, S + 4, 0, G);
      if (g % 6 == 1 && S + 5 < 80) p.add(E_AF, S + 5, 0, G);
      if ((m & 1) == 0) {
        if (g >= 24 && g < 32) p.add(E_SRD, m, g - 24, G);
        if (g == 35) p.add(E_WL, E_SRD, m, G);
        if (g >= 36 && g < 43) p.add(E_SADD, m, g - 35, G);
        if (g >= 43 && g < 50) p.add(E_SFIN, m, g - 43, G);
      }
      if (g >= 51 && g < 57) p.add(E_KK, m, g - 51, G);
      if (m == 1 && g >= 24 && g < 48 && (g & 1)) p.add(E_INIT, (g - 24) / 2, 0, G);
      for (int i = 0; i < NE4; ++i) {
        const int pos = 2 + i * (W4END - 26) / NE4;
        if (G == (pos < HCHK - 2 ? pos : pos + 24)) { if (i < NL4 - NL_PRE) p.add(E_W4L, NL_PRE + i, 0, G); else p.add(E_W4R, i - (NL4 - NL_PRE), 0, G); }
      }
      for (int i = 0; i < NF4; ++i) if (G == (i < 5 ? 112 + i * 8 : 168 - 10 + i * 2)) p.add(E_W4F, i, 0, G);
      if (m == 3) for (int i = 0; i < ND4; ++i) if (g == 4 + i * 2) p.add(E_W4D, i, 0, G);
      if (g % 6 == 5 && G < 239) p.add(E_WL, E_AF, S + 2, G);
    } else {
      const int m = B - 4;
      if (m == 0 && g >= NR4 - 1 && g < NR4 + NL4 + NF4 - 1 && (g - NR4 + 1) % 5 == 0) p.add(E_WL, E_UNP, imin(g - NR4 + 5, NL4 + NF4 - 1), G);
      if (m == 0 && g == 56) p.add(E_BARR, -1, 0, G);
      if (m == 1 && g < 10 && (g & 1) == 0) p.add(E_RW, 4, g / 2, G);
      if (m > 0) {
        if (g >= 9 && g < 19 && (g & 1) == 1) p.add(E_RW, m - 1, (g - 9) / 2, G);
        if (g == 22) p.add(E_BARR, m - 1, 0, G);
        if (g >= 23 && g < 33) p.add(E_RR, m - 1, g - 23, G);
        if (g == 37) p.add(E_WL, E_RR, m - 1, G);
        if (g >= 38 && g < 56) p.add(E_RADD, m - 1, g - 38, G);
        if (g == 56) p.add(E_OUT, m - 1, 0, G);
      }
      if (G == 477 || G == 478) p.add(E_RR, 3, G - 477 + 8, G);
      if (G == 479) {
        p.add(E_WL, -1, 0, G);
        for (int n = 0; n < 5; ++n) p.add(E_RW, 3, n, G);
        p.add(E_BARR, 3, 0, G);
        for (int u = 0; u < 8; ++u) p.add(E_RR, 3, u, G);
        p.add(E_WL, E_RR, 3, G);
        for (int u = 0; u < 18; ++u) p.add(E_RADD, 3, u, G);
        p.add(E_OUT, 3, 0, G);
      }
    }
    for (int i = 0; i < NL4 + NF4; ++i) if (G == UNP0 + i * (278 - UNP0) / (NL4 + NF4)) p.add(E_UNP, i, 0, G);
    for (int m = 0; m < 4; ++m) {
      for (int st = 0; st < 23; ++st) if (G == (m + 1) * 60 + 3 + st * (ACTE - 2) / 23) p.add(E_ACT, m, st, G);
      if (G == (m + 1) * 60 + ACTE + FLAGD) p.add(E_GFLAG, m, 0, G);
      if (G == (m + 3) * 60 + G4CHK - FLEAD) p.add(E_FLD, m, 0, G);
      if (G == (m + 3) * 60 + G4CHK) p.add(E_CHK, m, 0, G);
      for (int s2 = 0; s2 < 12; ++s2) if (G == (m + 3) * 60 + G4CHK + s2 * AGS / 12) p.add(E_AG, m, s2, G);
    }
  }
  p.first[481] = (short)p.n;
  return p;
}
constexpr Evs FP = ffn_prog();
constexpr unsigned L_P4 = 81920, L_HS = 159744, RED_W = 5376, RED_B = 4 * RED_W;

typedef u64 u64x8 __attribute__((ext_vector_type(8)));
typedef u64 u64x2 __attribute__((ext_vector_type(2)));
DI void pins(unsigned& a) { asm volatile("" : "+s"(a)); }
DI void pinv(unsigned& a) { asm volatile("" : "+v"(a)); }
DI unsigned ssel(bool c, unsigned a, unsigned b) { pins(a); pins(b); return c ? a : b; }
DI unsigned vsel(bool c, unsigned a, unsigned b) { pinv(a); pinv(b); return c ? a : b; }

__global__ __launch_bounds__(256, 1) void fnet_kernel(const u16* x_, const u16* wsr_, const u16* wsi_, const u16* wdr_, const u16* wdi_,
    const u16* w1_, const u16* b1_, const u16* w2_, const u16* b2_, u16* out_, char* ws_) {
  u64x8 ka; u64x2 kb; u64 kc;
  asm volatile("s_load_dwordx16 %0, %3, 0x0\n\ts_load_dwordx4 %1, %3, 0x40\n\ts_load_dwordx2 %2, %3, 0x50"
               : "=&s"(ka), "=&s"(kb), "=&s"(kc) : "s"(__builtin_amdgcn_kernarg_segment_ptr()));
  __builtin_amdgcn_sched_barrier(0);
  extern __shared__ __attribute__((aligned(16))) char smem[];
  const unsigned lds0 = (unsigned)(size_t)(LAS char*)smem;
  const int lane = threadIdx.x & 63;
  const int w = __builtin_amdgcn_readfirstlane(threadIdx.x >> 6);
  const int b = blockIdx.x, xg = b & 7, bi = b >> 3;
  const int q = lane >> 4, i16 = lane & 15;
  unsigned pf;
  {
    u64 pc; unsigned len;
    asm volatile("s_getpc_b64 %0\n.Lfnet_pc:\n\ts_mov_b32 %1, .Lfnet_end-.Lfnet_pc" : "=s"(pc), "=s"(len));
    const unsigned t = threadIdx.x, vc = (t & 60) == 0 ? (bi * 16 + (t >> 4) + (t & 3)) * 128 : 0xF0000000u;
    asm volatile("buffer_load_dword %0, %1, %2, 0 offen" : "=v"(pf) : "v"(vc), "s"(mk_rsrc((const void*)pc, len)) : "memory");
  }
  bf16x8 ones = {0x3F80, 0x3F80, 0x3F80, 0x3F80, 0x3F80, 0x3F80, 0x3F80, 0x3F80};
  const unsigned swz = i16 * 128 + ((q ^ (i16 >> 1)) * 16);

  const int pa = (xg >> 1) * 4 + (bi >> 3), j1 = bi & 7, part = xg & 1;
  const int ksp = xg >> 1, p128 = (xg & 1) * 8 + (bi >> 2), c4 = bi & 3;
  const int p = p128 * 2 + (ksp & 1), j = c4 * 2 + (ksp >> 1);
  const bool is2 = p >= 16;
  const int rr = 2 * w + (lane >> 5);
  const unsigned mW = lds0 + w * 4096;
  unsigned vx = (pa * 128 + (part * 8 + j1) * 8 + rr) * 1280 + (lane & 31) * 8, vw = (j1 * 80 + pa * 5 + (rr < 5 ? rr : 4)) * 1280 + (lane & 31) * 8;
  unsigned zero = 0, voA[4], voB[5];
#pragma unroll
  for (int i = 0; i < 4; ++i) { voA[i] = (8 * i + (lane >> 3)) * 1280 + (((lane & 7) ^ ((4 * i + q) & 7)) * 16) - 1024 * i; pinv(voA[i]); }
#pragma unroll
  for (int u = 0; u < 5; ++u) { voB[u] = (8 * u + (lane >> 3)) * 1280 + (((lane & 7) ^ ((4 * (w + u) + q) & 7)) * 16) - (u < 3 ? u : u - 3) * 1024; pinv(voB[u]); }
  unsigned soA = (pa * 128 + w * 32) * 1280, soB = (j1 * 80 + (w & 1) * 40) * 1280 + (w >> 1) * 128;
  unsigned mB1 = lds0 + L_B + w * 5120;
  pinv(vx); pinv(vw); pinv(zero); pins(soA); pins(soB); pins(mB1);
  __builtin_amdgcn_sched_barrier(0);
  asm volatile("s_waitcnt lgkmcnt(0)" ::: "memory");
  __builtin_amdgcn_sched_barrier(0);
  asm volatile("" : "+s"(ka), "+s"(kb), "+s"(kc));
  const u16* const x = (const u16*)ka[0]; const u16* const wd = (const u16*)(part ? ka[4] : ka[3]);
  char* const ws = (char*)kc;
  u32x2 xr[5], wr[5], epv, bias1[3], bias2[5];
  asm volatile("global_load_dwordx2 %0, %5, %6\n\tglobal_load_dwordx2 %1, %5, %6 offset:256\n\tglobal_load_dwordx2 %2, %5, %6 offset:512\n\t"
               "global_load_dwordx2 %3, %5, %6 offset:768\n\tglobal_load_dwordx2 %4, %5, %6 offset:1024"
               : "=&v"(xr[0]), "=&v"(xr[1]), "=&v"(xr[2]), "=&v"(xr[3]), "=&v"(xr[4]) : "v"(vx), "s"(x) : "memory");
  asm volatile("global_load_dwordx2 %0, %5, %6\n\tglobal_load_dwordx2 %1, %5, %6 offset:256\n\tglobal_load_dwordx2 %2, %5, %6 offset:512\n\t"
               "global_load_dwordx2 %3, %5, %6 offset:768\n\tglobal_load_dwordx2 %4, %5, %6 offset:1024"
               : "=&v"(wr[0]), "=&v"(wr[1]), "=&v"(wr[2]), "=&v"(wr[3]), "=&v"(wr[4]) : "v"(vw), "s"(wd) : "memory");
  asm volatile("global_load_dwordx2 %0, %1, %2 sc1" : "=&v"(epv) : "v"(zero), "s"(ws) : "memory");
  const rsrc_t rws = mk_rsrc(ws, WS_END), rx = mk_rsrc(x, S * D * 2), ra2 = mk_rsrc((const void*)(ksp < 2 ? ka[1] : ka[2]), S * S * 2);
  u16* const out = (u16*)kb[1];
  const unsigned soA2 = (p128 * 128 + w * 32) * 4096 + (ksp & 1) * 2048;
  auto tl = [&](int t) { return (unsigned)((t & ~3) + ((t + c4 + p128) & 3)); };
  unsigned voA2[4];
  auto wsPiece = [&](auto ic) {
    constexpr int i = ic.value;
    if constexpr (i < 4) { voA2[i] = voA[i]; pinv(voA2[i]); voA2[i] += (8 * i + (lane >> 3)) * (4096 - 1280); }
    if constexpr (i < 4 || i % 4 == 0) set_m0n<a2slot(i / 4)>(mW);
    dma<(i % 4) * 1024, 0>(voA2[i % 4], ra2, soA2 + tl(i / 4) * 128);
  };
  u64 pw1 = ka[5], pw2 = ka[7];
  asm volatile("" : "+s"(pw1), "+s"(pw2));
  unsigned rtS, rtD, rtSs, rtDs;
  f32x4 wd4[4];
  u64 ep;
  const unsigned vPr = ((((ksp & 1) * 8 + (lane & 7)) * 16) + (ksp >> 1) * 8 + 2 * c4 + ((lane >> 3) & 1)) * 128;
  u32x2 fa0;

  {
    const rsrc_t rb = mk_rsrc(wd, D * D * 2);
    const unsigned mA = mW + L_A, mB = mB1;
    const unsigned mS = mW + L_T;
    const unsigned csb = WS_CS + (part * 640 + j1 * 80) * 8;
    unsigned voS0, voS1;
    unsigned vPW;
    auto emit = [&](auto ic) {
      constexpr Ins e = G1P.v[ic.value];
      if constexpr (e.kind == K_A) {
        if constexpr (setm0(G1P, ic.value)) set_m0n<(e.idx & 3) * 16384 - (e.idx & 1) * 128>(mA);
        dma<e.sub * 1024 + (e.idx & 1) * 128, 0>(voA[e.sub], rx, soA + (e.idx >> 1) * 256);
      } else if constexpr (e.kind == K_B) {
        if constexpr (setm0(G1P, ic.value)) set_m0n<(e.idx % 3) * 20480 + (e.sub >= 3 ? 3072 : 0)>(mB);
        dma<(e.sub < 3 ? e.sub : e.sub - 3) * 1024, 0>(voB[e.sub], rb, soB + e.idx * 256);
      } else if constexpr (e.kind == K_PW) {
        put_flag(rws, vPW, ~ep);
      } else if constexpr (e.kind == K_WS) {
        wsPiece(IC<e.idx * 4 + e.sub>{});
      } else {
        if constexpr (e.sub == 0) {
          unsigned l = lane; asm volatile("" : "+v"(l));
          voS0 = l * 16 + (l < 32 ? WS_ST + (pa * 128 + w * 32) * 16 : csb - 512); voS1 = l < 8 ? csb - 512 + l * 16 : 0xF0000000u;
          set_m0(mS); dma<0, 2>(voS0, rws, 0u);
        } else dma<1024, 2>(voS1, rws, 0u);
      }
    };
    static_for<0, NPRE0>(emit);
    const unsigned rdA0 = mA + swz, rdA1 = rdA0 ^ 64, rdB0 = lds0 + L_B + swz, rdB1 = rdB0 ^ 64;
    {
      unsigned l = lane; pinv(l);
      const unsigned g = l >> 3, tile = (p128 * 2 + (w & 1)) * 8 + c4 * 2 + (w >> 1);
      unsigned o0 = WS_FP + (tile * 4 + ksp) * 256, o2 = WS_FH + ((p * 4 + w) * 8 + j) * 128, o3 = WS_FG + (p * 128 + j * 4 + w - 96) * 128;
      unsigned o7 = ssel(w == 0, WS_FAB + (pa * 16 + part * 8 + j1) * 128, ssel(w == 1, WS_F1 + (j * 32 + p) * 128, 0xF0000000u));
      pins(o0); pins(o2); pins(o3); pins(o7);
      unsigned o = o3 + g * 4096;
      o = g < 2 ? o0 + g * 128 : o; pinv(o); o = g == 2 ? o2 : o; pinv(o); o = g == 7 ? o7 : o;
      vPW = o + (l & 7) * 16; pinv(vPW);
      const unsigned kg = ssel(is2, ((p - 16) * 4 + (j & 3)) * 3, p * 5), cg = ssel(is2, (j >> 2) * 5, j * 3), sk = is2 ? 10240u : 24576u;
      const bool alongk = is2 == (w == 3);
      const unsigned k0 = kg + ssel(alongk, 0, w < 3 ? w : 4), c0 = cg + ssel(alongk, w < 3 ? w : 4, 0);
      rtS = k0 * sk + c0 * 128; rtD = (is2 ? WS_W28 : WS_W18) + k0 * sk + c0 * 1024; rtSs = alongk ? sk : 128u; rtDs = alongk ? sk : 1024u;
      pins(rtS); pins(rtD); pins(rtSs); pins(rtDs);
    }
    wait_vm<NPRE0>();
    asm volatile("" : "+v"(xr[0]), "+v"(xr[1]), "+v"(xr[2]), "+v"(xr[3]), "+v"(xr[4]), "+v"(wr[0]), "+v"(wr[1]), "+v"(wr[2]), "+v"(wr[3]), "+v"(wr[4]), "+v"(epv), "+v"(pf));
    ep = (((u64)(unsigned)__builtin_amdgcn_readfirstlane(epv.y) << 32) | (unsigned)__builtin_amdgcn_readfirstlane(epv.x)) + 1;
    const unsigned tag = (unsigned)ep;
    {
      constexpr int NR = G1P.lo(0) - NPRE0;
      auto more = [&](auto ic) { static_for<NPRE0 + ic.value * NR / 8, NPRE0 + (ic.value + 1) * NR / 8>(emit); };
      auto pinf = [](float& f) { asm volatile("" : "+v"(f)); };
      float v[20], s = 0.f, sw = 0.f, qq = 0.f;
      more(IC<0>{});
#pragma unroll
      for (int i = 0; i < 5; ++i) {
        v[4 * i] = bf_lo(xr[i].x); v[4 * i + 1] = bf_hi(xr[i].x); v[4 * i + 2] = bf_lo(xr[i].y); v[4 * i + 3] = bf_hi(xr[i].y);
        s += (v[4 * i] + v[4 * i + 1]) + (v[4 * i + 2] + v[4 * i + 3]);
        if (i == 2) { pinf(s); more(IC<1>{}); }
      }
      pinf(s); more(IC<2>{});
      float mean = xadd16(sum16(s)) * (1.f / D);
      pinf(mean); more(IC<3>{});
#pragma unroll
      for (int i = 0; i < 20; ++i) { const float d = v[i] - mean; qq += d * d; if (i == 9) { pinf(qq); more(IC<4>{}); } }
      pinf(qq); more(IC<5>{});
      float rstd = __builtin_amdgcn_rsqf(xadd16(sum16(qq)) * (1.f / D) + 1e-5f);
      pinf(rstd); more(IC<6>{});
      asm volatile("" : "+v"(wr[0]), "+v"(wr[1]), "+v"(wr[2]), "+v"(wr[3]), "+v"(wr[4]));
#pragma unroll
      for (int i = 0; i < 5; ++i) sw += (bf_lo(wr[i].x) + bf_hi(wr[i].x)) + (bf_lo(wr[i].y) + bf_hi(wr[i].y));
      pinf(sw); more(IC<7>{});
      sw = xadd16(sum16(sw));
      const bool lead = (lane & 31) == 0;
      st_sc1((u32x4){__float_as_uint(mean), tag, __float_as_uint(rstd), tag}, rws, lead ? WS_ST + (pa * 128 + (part * 8 + j1) * 8 + rr) * 16 : 0xF0000000u, 0);
      __builtin_amdgcn_raw_buffer_store_b64((u32x2){__float_as_uint(sw), tag}, rws, lead && rr < 5 ? csb + (pa * 5 + rr) * 8 : 0xF0000000u, 0, 16);
      asm volatile("" ::: "memory");
    }
    f32x4 acc[2][5];
    bf16x8 fa[2][2], fb[2][5], tg0, tg1;
    u32x4 sg[8];
    float cs[5];
    wait_vm<g1_wait(0)>();
    BARRIER();
    lds_rd<0>(fa[0][0], rdA0); lds_rd<0>(fb[0][0], rdB0); lds_rd<2048>(fa[0][1], rdA0); lds_rd<2048>(fb[0][1], rdB0);
    lds_rd<4096>(fb[0][2], rdB0); lds_rd<6144>(fb[0][3], rdB0); lds_rd<8192>(fb[0][4], rdB0);
    bf16x8 ta[4][2], tb[2][4];
    auto rdTb = [&](auto nc, auto kc) { constexpr int n = nc.value, k = kc.value; lds_rd<20480 + (k >> 1) * 10240 + n * 2048>(tb[n & 1][k], (k & 1) ? rdB1 : rdB0); };
    static_for<0, 16>([&](auto ic) {
      constexpr int h = ic.value, c = h & 1, t = h / 2;
      constexpr int h1 = h + 1, t1 = h1 / 2;
      constexpr int oA = (t1 & 3) * 16384, oB = ((t1 / 2) % 3) * 20480 + (t1 & 1) * 10240;
      const unsigned a1 = (h1 & 1) ? rdA1 : rdA0, b1r = (h1 & 1) ? rdB1 : rdB0;
      constexpr int lo = G1P.lo(h), cnt = G1P.lo(h + 1) - lo;
      if constexpr (c == 1) {
        wait_vm_lgkm0<g1_wait(h)>();
        if constexpr (t & 1) { __builtin_amdgcn_s_barrier(); asm volatile("" ::: "memory"); }
      } else wait_lgkm<0>();
      if constexpr (h == 14) {
        asm volatile("" : "+v"(tg0), "+v"(tg1));
#pragma unroll 1
        for (unsigned it = 0; it < SPIN_MAX; ++it) {
          const u32x4 u = __builtin_bit_cast(u32x4, tg0), v = __builtin_bit_cast(u32x4, tg1);
          if (__builtin_expect(__builtin_amdgcn_ballot_w64(((u.y ^ tag) | (u.w ^ tag) | (v.y ^ tag) | (v.w ^ tag)) != 0) == 0, 1)) break;
          unsigned keep;
          asm volatile("s_mov_b32 %0, m0" : "=s"(keep));
          set_m0(mS); dma<0, 2>(voS0, rws, 0u); dma<1024, 2>(voS1, rws, 0u); WAIT_VM(0);
          set_m0(keep);
          tg0 = lds_ld(mS + lane * 16); tg1 = lds_ld(mS + 1024 + (lane & 7) * 16);
        }
      }
      static_for<0, 10>([&](auto jc) {
        constexpr int i = jc.value;
        mfma_w<false, h == 0>(acc[i & 1][i >> 1], fa[c][i & 1], fb[c][i >> 1]);
        if constexpr (h < 15) {
          if constexpr (i == 0) { lds_rd<oA>(fa[c ^ 1][0], a1); lds_rd<oB>(fb[c ^ 1][0], b1r); }
          if constexpr (i == 1) { lds_rd<oA + 2048>(fa[c ^ 1][1], a1); lds_rd<oB + 2048>(fb[c ^ 1][1], b1r); }
          if constexpr (i == 2) { lds_rd<oB + 4096>(fb[c ^ 1][2], b1r); lds_rd<oB + 6144>(fb[c ^ 1][3], b1r); }
          if constexpr (i == 3) lds_rd<oB + 8192>(fb[c ^ 1][4], b1r);
        } else if constexpr (i < 8) {
          lds_rd<(i >> 2) * 16384 + (i & 1) * 2048>(ta[i >> 1][i & 1], (i & 2) ? rdA1 : rdA0);
          if constexpr (i & 1) rdTb(IC<0>{}, IC<(i >> 1)>{});
        }
        if constexpr (h == 13 && i == 4) { lds_rd<0>(tg0, mS + lane * 16); lds_rd<1024>(tg1, mS + (lane & 7) * 16); }
        if constexpr (h == 14 && i >= 4) {
          if constexpr (i < 8) { sg[2 * i - 8] = *(LAS u32x4*)(mS + (16 * ((i - 4) >> 1) + 4 * q + ((2 * i) & 3)) * 16); sg[2 * i - 7] = *(LAS u32x4*)(mS + (16 * ((i - 4) >> 1) + 4 * q + ((2 * i + 1) & 3)) * 16); }
          if constexpr (i < 9) cs[i - 4] = *(LAS float*)(mS + 512 + (16 * (i - 4) + i16) * 8);
        }
        if constexpr (i >= 3 && i < 9) static_for<lo + (i - 3) * cnt / 6, lo + (i - 2) * cnt / 6>(emit);
      });
    });
    unsigned vo = WS_AB8 + part * (S * D * 2) + (pa * 16 + w * 4 + (q & 1) * 2 + (q >> 1)) * 10240 + (j1 * 80 + i16) * 16;
    pinv(vo);
    float ev[8]; unsigned ed[4];
    auto fin = [&](auto nc, auto sc) {
      constexpr int n = nc.value, st = sc.value;
      if constexpr (st < 3) { static_for<3 * st, imin(3 * st + 3, 8)>([&](auto ec) { constexpr int e = ec.value; v_nfma(ev[e], __uint_as_float(sg[e].x), cs[n], acc[e >> 2][n][e & 3]); }); }
      else if constexpr (st < 6) { static_for<3 * st - 9, imin(3 * st - 6, 8)>([&](auto ec) { constexpr int e = ec.value; v_mul(ev[e], __uint_as_float(sg[e].z)); }); if constexpr (st == 5) v_cvt(ed[0], ev[0], ev[1]); }
      else if constexpr (st == 6) { v_cvt(ed[2], ev[4], ev[5]); v_cvt(ed[1], ev[2], ev[3]); v_cvt(ed[3], ev[6], ev[7]); }
      else {
        v_swap16(ed[0], ed[2]); asm volatile("s_nop 0"); v_swap16(ed[1], ed[3]);
        st_sc1((u32x4){ed[0], ed[1], ed[2], ed[3]}, rws, vo + n * 256, 0); asm volatile("" ::: "memory");
      }
    };
    static_for<0, 5>([&](auto nc) {
      constexpr int n = nc.value;
      wait_lgkm<0>();
      static_for<0, 8>([&](auto jc) {
        constexpr int i = jc.value;
        mfma_vv(acc[i & 1][n], ta[i >> 1][i & 1], tb[n & 1][i >> 1]);
        if constexpr (n < 4 && i < 4) rdTb(IC<n + 1>{}, IC<i>{});
        if constexpr (n > 0 && i >= 3) fin(IC<n - 1>{}, IC<i - 3>{});
        if constexpr (n > 1 && i < 3) fin(IC<n - 2>{}, IC<i + 5>{});
      });
    });
    fin(IC<3>{}, IC<5>{}); fin(IC<3>{}, IC<6>{}); fin(IC<3>{}, IC<7>{});
    asm volatile("s_nop 1");
    static_for<0, 8>([&](auto sc) { fin(IC<4>{}, sc); });
    asm volatile("" ::: "memory");
  {
    const rsrc_t rw = mk_rsrc((const void*)(is2 ? pw2 : pw1), D * F * 2);
    const unsigned vo = (lane >> 3) * (is2 ? 1280u : 3072u) + (lane & 7) * 16;
    static_for<0, 4>([&](auto ic) { set_m0n<L_R + ic.value * 1024>(mW); dma<0, 0>(vo, rw, ic.value == 3 && w == 3 ? 0x70000000u : rtS + ic.value * rtSs); });
#pragma unroll
    for (int n = 0; n < 3; ++n) bias1[n] = __builtin_amdgcn_raw_buffer_load_b64(mk_rsrc((const void*)ka[6], F * 2), q * 8 + n * 32, (j * 192 + w * 48) * 2, 0);
#pragma unroll
    for (int n = 0; n < 5; ++n) bias2[n] = __builtin_amdgcn_raw_buffer_load_b64(mk_rsrc((const void*)kb[0], D * 2), q * 8 + n * 32, j * 160, 0);
    asm volatile("" ::: "memory");
  }
    wait_vm<12>();
    BARRIER();
    put_flag(rws, flag_off(lane, w == 0, WS_FAB + (pa * 16 + part * 8 + j1) * 128), ep);
    static_for<0, 4>([&](auto ic) { ld_uncounted<ic.value * 1024>(wd4[ic.value], ra2, voA2[ic.value], soA2 + tl(4) * 128); });
    static_for<4, 16 - NAFT>(wsPiece);
    ld64_sc1_uncounted(fa0, rws, vPr, WS_FAB);
    static_for<16 - NAFT, 16>(wsPiece);
  }

  bf16x8 W3[60], W4[60];
  const unsigned vW = q * 10240 + i16 * 16, vW3 = q * 24576 + i16 * 16;
  const int rot3 = (4 * c4 + ((5 * (p128 & 7)) >> 1)) % 20, rot4 = (3 * (p128 & 7)) >> 1;
  auto ldW3 = [&](auto ic) { constexpr int i = ic.value, ks = i / 3; const int k = ks + rot3 >= 20 ? ks + rot3 - 20 : ks + rot3;
    W3[i] = ld_sc1(rws, vW3 + (i % 3) * 256, WS_W18 + k * 98304 + (j * 192 + w * 48) * 16); asm volatile("" ::: "memory"); };
  unsigned s4[12];
  auto ldW4 = [&](auto ic) { constexpr int i = ic.value; W4[i] = ld_sc1(rws, vW + (i % 5) * 256, s4[i / 5]); asm volatile("" ::: "memory"); };

  f32x4 y[5];
  u32x2 fp0;
  const unsigned vPF = (ksp ^ (1 + (lane & 1) + ((lane >> 1) & 1))) * 256, sPF = WS_FP + (p * 8 + j) * 1024 + (w >> 1) * 128;
  const float sgo = ksp < 2 ? 1.f : -1.f;
  {
    unsigned voB[5];
    const unsigned mB = lds0 + w * 5120;
#pragma unroll
    for (int u = 0; u < 5; ++u) { const int g = 64 * u + lane; voB[u] = (g >= 160 ? 10240 + (g - 160) * 16 : g * 16) - (u < 3 ? u : u - 3) * 1024; }
    unsigned soB = WS_AB8 + (ksp >> 1) * (S * D * 2) + ((ksp & 1) * 128 + 2 * w) * 10240 + c4 * 2560;
    const unsigned rdA0 = lds0 + (w & 1) * 8192 + swz, rdA1 = rdA0 ^ 64, rdA2 = rdA0 + a2slot(0), rdA3 = rdA1 + a2slot(0);
    const unsigned rdB0 = lds0 + b2slot(0) + q * 2560 + ((w >> 1) * 80 + i16) * 16, rdB2 = rdB0 + (b2slot(3) - b2slot(0));
    const unsigned wrA = mW + a2slot(0) + lane * 16;
    unsigned voX[3];
#pragma unroll
    for (int u = 0; u < 3; ++u) { const unsigned g = u * 64 + lane, r = (g * 6554u) >> 16; voX[u] = (r * 1280 + (g - r * 10) * 16 - u * 1024) | (g < 160 ? 0u : 0xF0000000u); }
    u32x4 rt[4]; unsigned dstR[4];
    auto rt_read = [&](auto ic) {
      constexpr int u = ic.value;
      const unsigned d0 = u == 3 && w == 3 ? 0xE0000000u : rtD + u * rtDs;
      const unsigned ta = mW + L_R + (i16 >> 2) * 128 + q * 32 + (lane & 3) * 8;
      dstR[u] = d0 + lane * 16;
      rt[u] = __builtin_bit_cast(u32x4, __builtin_shufflevector(lds_tr(ta + u * 1024), lds_tr(ta + u * 1024 + 512), 0, 1, 2, 3, 4, 5, 6, 7));
      asm volatile("" : "+v"(dstR[u]) :: "memory");
    };
    const unsigned vF1 = vsel(q == 0, ((j & 4) + w) * 32 + 16 + i16, j * 32 + w * 4 + (lane & 3)) * 128;
    u32x2 fav;
    f32x4 acc[4][5];
    const unsigned tile = (p128 * 2 + (w & 1)) * 8 + c4 * 2 + (w >> 1);
    const unsigned po = (w == ksp ? 0xF0000000u : WS_PART + (ksp * 256 + tile) * 10240) + lane * 16, po2 = po + 2048 - lane * 8;
    const unsigned lo16 = lds0 + b2slot(0) + lane * 16;
    const u64 own = __builtin_amdgcn_ballot_w64(w == ksp);
    u32x4 pk[4][3];
    auto put = [&](auto mc, auto sc) {
      constexpr int m = mc.value, s = sc.value;
      if constexpr (s < 2) {
        pin(acc[m][2 * s]); pin(acc[m][2 * s + 1]);
        const f32x4 u = acc[m][2 * s], v = acc[m][2 * s + 1];
        pk[m][s] = (u32x4){pk_bf16(u[0], u[1]), pk_bf16(u[2], u[3]), pk_bf16(v[0], v[1]), pk_bf16(v[2], v[3])};
        st_sc1(pk[m][s], rws, po + m * 2560 + s * 1024, 0);
      } else {
        pin(acc[m][4]);
        pk[m][2].x = pk_bf16(acc[m][4][0], acc[m][4][1]); pk[m][2].y = pk_bf16(acc[m][4][2], acc[m][4][3]);
        __builtin_amdgcn_raw_buffer_store_b64((u32x2){pk[m][2].x, pk[m][2].y}, rws, po2 + m * 2560, 0, 16);
      }
    };
    auto keep = [&](auto ic) {
      constexpr int m = ic.value / 3, s = ic.value % 3;
      if constexpr (s < 2) lds_wr_u4<(m * 3 + s) * 1024>(lo16, pk[m][s], own); else lds_wr_u2<(m * 3 + s) * 1024>(lo16, (u32x2){pk[m][2].x, pk[m][2].y}, own);
    };
    const unsigned vFP = flag_off(lane, w != ksp, WS_FP + (tile * 4 + ksp) * 256);
    auto emit = [&](auto ic) {
      constexpr Ins e = G2P.v[ic.value];
      constexpr bool M = setm0(G2P, ic.value);
      if constexpr (e.kind == K_A) {
        if constexpr (M) set_m0n<a2slot(e.idx & 3)>(mW);
        dma<e.sub * 1024, 0>(voA2[e.sub], ra2, soA2 + tl(e.idx) * 128);
      } else if constexpr (e.kind == K_B) {
        if constexpr (M) set_m0n<b2slot(e.idx & 3) + (e.sub >= 3 ? 3072 : 0)>(mB);
        dma<(e.sub < 3 ? e.sub : e.sub - 3) * 1024, 2>(voB[e.sub], rws, soB + tl(e.idx) * 81920);
      } else if constexpr (e.kind == K_RS) {
        st_sc1(rt[e.sub], rws, dstR[e.sub], 0); asm volatile("" ::: "memory");
      } else if constexpr (e.kind == K_XT) {
        if constexpr (M) set_m0n<L_R>(mW);
        dma<e.sub * 1024, 0>(voX[e.sub], rx, (p * 64 + w * 16) * 1280 + j * 160);
      } else if constexpr (e.kind == K_F1) {
        ld64_sc1_uncounted(fav, rws, vF1, WS_F1);
      } else if constexpr (e.kind == K_PUT) {
        put(IC<e.sub / 3>{}, IC<e.sub % 3>{}); asm volatile("" ::: "memory");
      } else if constexpr (e.kind == K_FLAG) {
        wait_vm<ic.value - 1 - G2P.last(K_PUT, 0)>();
        put_flag(rws, vFP, ep);
      } else ldW3(IC<e.sub>{});
    };
    pinv(voB[0]); pinv(voB[1]); pinv(voB[2]); pinv(voB[3]); pinv(voB[4]); pins(soB);
    { unsigned t0 = soB + tl(0) * 81920; asm volatile("" :: "s"(t0)); }
    wait_vm<NAFT>();
    pin2(fa0);
    if (flag_missing(fa0, ep)) poll(rws, WS_FAB, vPr, ep);
    pin(wd4[0]); pin(wd4[1]); pin(wd4[2]); pin(wd4[3]);
    static_for<0, G2P.lo(0)>(emit);
    static_for<0, 4>(rt_read);
    bf16x8 fa[2][4], fb[2][5];
    wait_vm<g2_wait(0)>();
    BARRIER();
    lds_rd<0>(fb[0][0], rdB0); lds_rd<0>(fa[0][0], rdA2); lds_rd<2048>(fa[0][1], rdA2); lds_rd<4096>(fa[0][2], rdA2); lds_rd<6144>(fa[0][3], rdA2);
    lds_rd<256>(fb[0][1], rdB0); lds_rd<512>(fb[0][2], rdB0); lds_rd<768>(fb[0][3], rdB0); lds_rd<1024>(fb[0][4], rdB0);
    static_for<0, HA>([&](auto ic) {
      constexpr int h = ic.value, c = h & 1;
      constexpr int h1 = h + 1, s1 = (h1 / 2) & 3;
      constexpr int oS = s1 == 0 ? 0 : a2slot(s1), oB = (s1 == 3 ? 0 : s1 * 20480) + (h1 & 1) * 10240;
      const unsigned a1 = s1 == 0 ? ((h1 & 1) ? rdA3 : rdA2) : ((h1 & 1) ? rdA1 : rdA0), b1r = s1 == 3 ? rdB2 : rdB0;
      constexpr int lo = G2P.lo(h), cnt = G2P.lo(h + 1) - lo;
      if constexpr (c == 1) {
        wait_vm_lgkm0<g2_wait(h)>();
        if constexpr (h == F1_CHK) {
          pin2(fav);
          if (__builtin_expect(flag_missing(fav, ep), 0)) poll(rws, WS_F1, vF1, ep);
        }
        __builtin_amdgcn_s_barrier();
        asm volatile("" ::: "memory");
        if constexpr (h == F1_SET) put_flag(rws, flag_off(lane, w == 0, WS_F1 + (j * 32 + p) * 128), ep);
      } else wait_lgkm<4>();
      static_for<0, 20>([&](auto jc) {
        constexpr int i = jc.value, n = i >> 2, m = (n & 1) ? 3 - (i & 3) : i & 3;
        if constexpr (c == 0 && i == 4) wait_lgkm<4>();
        mfma_w<false, h == 0>(acc[m][n], fb[c][n], fa[c][m]);
        if constexpr (i == 0) lds_rd<oB>(fb[c ^ 1][0], b1r);
        if constexpr (i >= 1 && i <= (h < HA - 1 ? 3 : 2)) lds_rd<oS + (i - 1) * 2048>(fa[c ^ 1][i - 1], a1);
        if constexpr (i == 5 && h < HA - 1) lds_rd<oS + 6144>(fa[c ^ 1][3], a1);
        if constexpr (i == 7 || i == 9 || i == 11 || i == 13) lds_rd<oB + ((i - 5) / 2) * 256>(fb[c ^ 1][(i - 5) / 2], b1r);
        if constexpr (h == 2 && i >= 14 && i < 18) lds_wr_f4<((i - 14) & 3) * 1024>(wrA, wd4[(i - 14) & 3]);
        if constexpr (i < 19) static_for<lo + i * cnt / 19, lo + (i + 1) * cnt / 19>(emit);
      });
    });
    static_for<HA, HE>([&](auto ic) {
      constexpr int h = ic.value, c = h & 1, mb = h < 32 ? 0 : 2;
      constexpr int h1 = h + 1, mb1 = h1 < 32 ? 0 : 2, s1 = (h1 / 2) & 3;
      constexpr int oS = (s1 == 0 ? 0 : a2slot(s1)) + mb1 * 2048, oB = (s1 == 3 ? 0 : s1 * 20480) + (h1 & 1) * 10240;
      const unsigned a1 = s1 == 0 ? ((h1 & 1) ? rdA3 : rdA2) : ((h1 & 1) ? rdA1 : rdA0), b1r = s1 == 3 ? rdB2 : rdB0;
      constexpr int lo = G2P.lo(h), cnt = G2P.lo(h + 1) - lo;
      if constexpr (c == 1 && h < 30) wait_vm_lgkm0<g2_wait(h)>(); else wait_lgkm<0>();
      if constexpr ((c == 1 && h < 30) || h == 34) { __builtin_amdgcn_s_barrier(); asm volatile("" ::: "memory"); }
      static_for<0, 10>([&](auto jc) {
        constexpr int i = jc.value;
        constexpr int n = h == HE - 1 ? i % 5 : i >> 1, m = h == HE - 1 ? i / 5 : (n & 1) ? 1 - (i & 1) : i & 1;
        mfma_vv(acc[mb + m][n], fb[c][n], fa[c][m]);
        if constexpr (h < HE - 1) {
          if constexpr (i == 0) { lds_rd<oB>(fb[c ^ 1][0], b1r); lds_rd<oS>(fa[c ^ 1][0], a1); }
          if constexpr (i == 1) { lds_rd<oS + 2048>(fa[c ^ 1][1], a1); lds_rd<oB + 256>(fb[c ^ 1][1], b1r); }
          if constexpr (i >= 2 && i <= 4) lds_rd<oB + i * 256>(fb[c ^ 1][i], b1r);
        }
        if constexpr (h >= 35 && h < 38 && (i == 5 || i == 6)) keep(IC<(2 * (h - 35) + i - 5) % 12>{});
        if constexpr (i >= 4 && i < 9) static_for<lo + (i - 4) * cnt / 5, lo + (i - 3) * cnt / 5>(emit);
      });
    });
    put(IC<2>{}, IC<0>{}); put(IC<2>{}, IC<1>{});
    MFMA_DRAIN();
    put(IC<2>{}, IC<2>{}); put(IC<3>{}, IC<0>{}); put(IC<3>{}, IC<1>{}); put(IC<3>{}, IC<2>{});
    asm volatile("" ::: "memory");
    static_for<6, 12>(keep);
    static_for<N3_TAIL, N3_TAIL + N3_PP>(ldW3);
    ld64_sc1_uncounted(fp0, rws, vPF, sPF);
    u32x2 xt[5];
#pragma unroll
    for (int n = 0; n < 5; ++n) xt[n] = *(LAS u32x2*)(mW + L_R + i16 * 160 + n * 32 + q * 8);
    BARRIER();
    {
      const unsigned ob = lds0 + b2slot(0) + w * 3072 + lane * 16;
      const u32x4 o0 = *(LAS u32x4*)ob, o1 = *(LAS u32x4*)(ob + 1024); const u32x2 o2 = *(LAS u32x2*)(ob + 2048);
#pragma unroll
      for (int n = 0; n < 5; ++n) {
        const unsigned d0 = n < 2 ? o0[2 * n] : n < 4 ? o1[2 * n - 4] : o2[0], d1 = n < 2 ? o0[2 * n + 1] : n < 4 ? o1[2 * n - 3] : o2[1];
        y[n] = (f32x4){bf_lo(xt[n].x), bf_hi(xt[n].x), bf_lo(xt[n].y), bf_hi(xt[n].y)} + sgo * (f32x4){bf_lo(d0), bf_hi(d0), bf_lo(d1), bf_hi(d1)};
        pin(y[n]);
      }
    }
    BARRIER();
#pragma unroll
    for (int ks = 0; ks < 12; ++ks) { s4[ks] = WS_W28 + (w * 12 + (ks + rot4 >= 12 ? ks + rot4 - 12 : ks + rot4)) * 40960 + j * 1280; pins(s4[ks]); }
    wait_vm<N3_PP + 1>();
    put_flag(rws, vFP + 128, ep);
    asm volatile("" ::: "memory");
  }

  unsigned hres[5][2];
  f32x4 cs[3];
  auto colsum = [&](auto ic) { constexpr int i = ic.value; mfma_w<true, (i < 3)>(cs[i % 3], W3[i], ones); };
  constexpr int N3_X = N3_TAIL + N3_PP + N3_R + N3_HP, NBULK = 60 - N3_X + NL_PRE;
  auto bulk = [&](auto ic) {
    constexpr int i = N3_X + ic.value;
    if constexpr (i < 60) {
      const int k = i / 3 + rot3 >= 20 ? i / 3 + rot3 - 20 : i / 3 + rot3;
      ld_sc1_a(W3[i], rws, vW3, WS_W18 + k * 98304 + (j * 192 + w * 48) * 16 + (i % 3) * 256);
    } else {
      constexpr int u = i - 60, f = NR4 + u;
      set_m0n<L_P4 + u * 1024 - (f % 5) * 256>(lds0 + w * 19456);
      dma<(f % 5) * 256, 2>(vW, rws, s4[f / 5]);
    }
  };
  {
    static_for<N3_X, 60>([&](auto ic) { undef_a(W3[ic.value]); });
    unsigned sP[3], l8 = lane * 8 + 2048;
#pragma unroll
    for (int k = 0; k < 3; ++k) { sP[k] = WS_PART + ((ksp ^ (k + 1)) * 256 + p * 8 + j) * 10240 + w * 2560; pins(sP[k]); }
    pinv(l8);
    if (w >= 2) { static_for<0, NB_PRE>(bulk); wait_vm<NB_PRE>(); } else wait_vm<1>();
    pin2(fp0);
    if (flag_missing(fp0, ep)) poll(rws, sPF, vPF, ep);
    u32x4 u0[3], u1[3]; u32x2 u2[3];
#pragma unroll
    for (int k = 0; k < 3; ++k) {
      const unsigned po = sP[k];
      u0[k] = __builtin_amdgcn_raw_buffer_load_b128(rws, lane * 16, po, 16);
      u1[k] = __builtin_amdgcn_raw_buffer_load_b128(rws, lane * 16 + 1024, po, 16);
      u2[k] = __builtin_amdgcn_raw_buffer_load_b64(rws, l8, po, 16);
    }
    asm volatile("" ::: "memory");
    static_for<N3_TAIL + N3_PP, N3_TAIL + N3_PP + N3_R>(ldW3);
#pragma unroll
    for (int n = 0; n < 5; ++n)
#pragma unroll
      for (int e = 0; e < 2; ++e) {
        unsigned d[3];
#pragma unroll
        for (int k = 0; k < 3; ++k) d[k] = n < 2 ? u0[k][2 * n + e] : n < 4 ? u1[k][2 * (n - 2) + e] : u2[k][e];
        hres[n][e] = pk_bf16(y[n][2 * e] + sgo * ((bf_lo(d[0]) - bf_lo(d[1])) - bf_lo(d[2])), y[n][2 * e + 1] + sgo * ((bf_hi(d[0]) - bf_hi(d[1])) - bf_hi(d[2])));
      }
    float s1 = 0.f, s2 = 0.f;
    const unsigned so = WS_H8 + p * 81920 + w * 20480 + j * 2560;
    const unsigned vo = ((q & 1) * 2 + (q >> 1)) * 256 + i16 * 16;
#pragma unroll
    for (int n = 0; n < 4; n += 2) {
      auto r0 = __builtin_amdgcn_permlane16_swap(hres[n][0], hres[n + 1][0], false, false);
      auto r1 = __builtin_amdgcn_permlane16_swap(hres[n][1], hres[n + 1][1], false, false);
      st_sc1((u32x4){r0[0], r1[0], r0[1], r1[1]}, rws, vo + n * 512, so);
    }
    {
      auto r0 = __builtin_amdgcn_permlane16_swap(hres[4][0], hres[4][0], false, false);
      auto r1 = __builtin_amdgcn_permlane16_swap(hres[4][1], hres[4][1], false, false);
      st_sc1((u32x4){r0[0], r1[0], r0[1], r1[1]}, rws, (q & 1) ? 0xF0000000u : so + (8 + (q >> 1)) * 256 + i16 * 16, 0);
    }
#pragma unroll
    for (int n = 0; n < 5; ++n)
#pragma unroll
      for (int e = 0; e < 2; ++e) {
        const bf16x2v hv = __builtin_bit_cast(bf16x2v, hres[n][e]);
        s1 = __builtin_amdgcn_fdot2_f32_bf16(hv, __builtin_bit_cast(bf16x2v, 0x3F803F80u), s1, false); s2 = __builtin_amdgcn_fdot2_f32_bf16(hv, hv, s2, false);
      }
    s1 = xadd32(xadd16(s1)); s2 = xadd32(xadd16(s2));
    __builtin_amdgcn_raw_buffer_store_b64((u32x2){__float_as_uint(s1), __float_as_uint(s2)}, rws, lane < 16 ? lane * 8 : 0xF0000000u, WS_HST + p * 4096 + w * 1024 + j * 128, 16);
    asm volatile("" ::: "memory");
    static_for<N3_X - N3_HP, N3_X>(ldW3);
    asm volatile("s_nop 1" : "+v"(ones));
    static_for<0, N3_TAIL + N3_PP>(colsum);
    wait_vm<N3_HP>();
    put_flag(rws, flag_off(lane, true, WS_FH + ((p * 4 + w) * 8 + j) * 128), ep);
  }

  {
    if (w < 2) static_for<0, NB_PRE>(bulk);
    static_for<NB_PRE, NBULK>(bulk);
    float bb[12];
    f32x4 y0[5];
#pragma unroll
    for (int n = 0; n < 3; ++n) {
      bb[4 * n] = bf_lo(bias1[n].x); bb[4 * n + 1] = bf_hi(bias1[n].x); bb[4 * n + 2] = bf_lo(bias1[n].y); bb[4 * n + 3] = bf_hi(bias1[n].y);
      asm volatile("" : "+v"(bb[4 * n]), "+v"(bb[4 * n + 1]), "+v"(bb[4 * n + 2]), "+v"(bb[4 * n + 3]));
    }
#pragma unroll
    for (int n = 0; n < 5; ++n) {
      y0[n] = (f32x4){bf_lo(bias2[n].x) + bf_lo(hres[n][0]), bf_hi(bias2[n].x) + bf_hi(hres[n][0]), bf_lo(bias2[n].y) + bf_lo(hres[n][1]), bf_hi(bias2[n].y) + bf_hi(hres[n][1])};
      pin(y0[n]);
    }
    unsigned l16 = lane * 16, rdH0 = lds0 + lane * 16, rdH1 = rdH0 + 40960, rdS = lds0 + L_HS + (q & 1) * 1024 + i16 * 8, rdP = lds0 + L_P4 + w * 19456 + lane * 16, rdF = lds0 + w * 5120 + lane * 16;
    unsigned vG = WS_G8 + p * 196608 + (j * 24 + w * 6) * 256 + ((q & 1) * 2 + (q >> 1)) * 256 + i16 * 16;
    unsigned vG2 = (q & 1) ? 0xF0000000u : WS_G8 + p * 196608 + (j * 24 + w * 6 + 4 + (q >> 1)) * 256 + i16 * 16;
    unsigned vF = flag_off(lane, true, WS_FG + (p * 128 + j * 4 + w) * 128), vC = (lane & 7) * 128;
    const unsigned tr = (lane * 6554u) >> 16, tc = lane - tr * 10;
    unsigned wrR = lds0 + w * RED_W + i16 * 336 + q * 16, rdR = lds0 + (w * 4 + (tr & 3)) * 336 + tc * 32;
    unsigned vO = lane < 40 ? (p * 64 + w * 4 + tr) * 1280 + j * 160 + tc * 16 : 0xF0000000u;
    pinv(l16); pinv(rdH0); pinv(rdH1); pinv(rdS); pinv(rdP); pinv(rdF); pinv(vG); pinv(vG2); pinv(vF); pinv(vC); pinv(wrR); pinv(rdR); pinv(vO);
    unsigned mF = lds0 + w * 5120, sH[5], sG[12], vA[4];
    pins(mF);
#pragma unroll
    for (int u = 0; u < 5; ++u) { const int k = w * 5 + u + rot3; sH[u] = WS_H8 + p * 81920 + 40960 + (k >= 20 ? k - 20 : k) * 1024; pins(sH[u]); }
    unsigned l16b = lane * 16 + 20480; pinv(l16b);
    const rsrc_t ro = mk_rsrc(out, S * D * 2);
    if (w < 2) {
      unsigned sA[10], mH = lds0 + w * 10240;
#pragma unroll
      for (int v = 0; v < 10; ++v) { const int k = w * 10 + v + rot3; sA[v] = WS_H8 + p * 81920 + (k >= 20 ? k - 20 : k) * 1024; pins(sA[v]); }
      pins(mH);
      poll(rws, WS_FH + p * 4096, i16 * 128, ep);
      static_for<0, 21>([&](auto ic) {
        constexpr int u = ic.value, m = u > 10, v = u < 10 ? u : u - 11;
        if constexpr (u == 10) { set_m0(lds0 + L_HS + w * 1024); dma<0, 2>(l16, rws, WS_HST + p * 4096 + w * 1024); }
        else {
          set_m0n<m * 20480 + v * 1024>(mH);
          dma<0, 2>(m ? l16b : l16, rws, sA[v]);
        }
      });
    } else __builtin_amdgcn_s_waitcnt(0x0F70);
    f32x4 a3[2][3], acc[2][5], rr4[10];
    f32x2 sr[8];
    float kk[12], s1, s2, mean, tt[4], o8[8];
    unsigned gp[4][2], op[4], rs[4], mr[2];
    bf16x8 af[6], ag[4][12];
    u32x2 fl;
    auto emit = [&](auto ic) {
      constexpr int I = ic.value;
      constexpr Ev e = FP.v[I];
      constexpr int A = e.a, Bv = e.b;
      if constexpr (e.kind == E_HD) {
        if constexpr (Bv < 5) {
          set_m0n<A * 20480 + Bv * 1024>(mF);
          dma<0, 2>(A == 2 ? l16 : l16b, rws, sH[Bv]);
        } else { set_m0(lds0 + L_HS + (A + (w & 1)) * 1024); dma<0, 2>(l16, rws, WS_HST + p * 4096 + (A + (w & 1)) * 1024); }
      } else if constexpr (e.kind == E_BARH) {
        if constexpr (A == 0) { if (threadIdx.x == 0 && b == 0) set_flag(ws, WS_CTL, ep);
                                static_for<N3_TAIL + N3_PP, 60>(colsum); }
        wait_vm<A == 0 ? 10 : FP.vm(FP.last(E_HD, A, -1, I), I)>();
        __builtin_amdgcn_s_barrier(); asm volatile("" ::: "memory");
      } else if constexpr (e.kind == E_AF) {
        if constexpr (A < 40) lds_rd<A * 1024>(af[A % 6], rdH0); else lds_rd<(A - 40) * 1024>(af[A % 6], rdH1);
      } else if constexpr (e.kind == E_WL) {
        if constexpr (A < 0) MFMA_DRAIN();
        else { static_assert(FP.last(A, Bv, -1, I) >= 0); wait_lgkm<FP.lds(FP.last(A, Bv, -1, I), I)>(); }
      } else if constexpr (e.kind == E_SRD) {
        lds_rd_f2<A * 1024 + Bv * 128>(sr[Bv], rdS);
      } else if constexpr (e.kind == E_SADD) {
        if constexpr (Bv == 1) { v_add2(s1, sr[0][0], sr[1][0]); v_add2(s2, sr[0][1], sr[1][1]); } else { v_add(s1, sr[Bv][0]); v_add(s2, sr[Bv][1]); }
      } else if constexpr (e.kind == E_SFIN) {
        if constexpr (Bv == 0) { mean = s1 * (1.f / D); s2 = s2 * (1.f / D); asm volatile("" : "+v"(mean), "+v"(s2)); }
        if constexpr (Bv == 1) { s2 = fmaxf(s2 - mean * mean, 0.f); asm volatile("" : "+v"(s2)); }
        if constexpr (Bv == 2) { s2 = __builtin_amdgcn_rsqf(s2 + 1e-5f); asm volatile("" : "+v"(s2)); }
        if constexpr (Bv == 3) { mean *= s2; asm volatile("" : "+v"(mean)); }
        if constexpr (Bv == 4) { rs[A] = rs[A + 1] = __float_as_uint(s2); mr[0] = mr[1] = __float_as_uint(mean); asm volatile("" : "+v"(rs[A]), "+v"(rs[A + 1]), "+v"(mr[0]), "+v"(mr[1])); }
        if constexpr (Bv == 6) { v_swap16(rs[A], rs[A + 1]); v_swap16(mr[0], mr[1]); }
      } else if constexpr (e.kind == E_KK) {
        v_nfma(kk[2 * Bv], __uint_as_float(mr[A & 1]), cs[Bv / 2][(2 * Bv) % 4], bb[2 * Bv]); v_nfma(kk[2 * Bv + 1], __uint_as_float(mr[A & 1]), cs[Bv / 2][(2 * Bv) % 4 + 1], bb[2 * Bv + 1]);
      } else if constexpr (e.kind == E_ACT) {
        constexpr int n = Bv < 14 ? Bv / 7 : 2, st = Bv < 14 ? Bv % 7 : Bv - 15;
        if constexpr (Bv == 14) {
          if constexpr (ACTE < 25) asm volatile("s_nop 0");
          v_swap16(gp[0][0], gp[1][0]); v_swap16(gp[0][1], gp[1][1]);
          st_sc1((u32x4){gp[0][0], gp[0][1], gp[1][0], gp[1][1]}, rws, vG + A * 49152, 0); asm volatile("" ::: "memory");
        } else if constexpr (Bv == 22) {
          asm volatile("s_nop 0");
          v_swap16(gp[2][0], gp[3][0]); v_swap16(gp[2][1], gp[3][1]);
          st_sc1((u32x4){gp[2][0], gp[2][1], gp[3][0], gp[3][1]}, rws, vG2 + A * 49152, 0); asm volatile("" ::: "memory");
        } else if constexpr (st < 2) { v_fma(tt[2 * st], a3[A & 1][n][2 * st], __uint_as_float(rs[A]), kk[4 * n + 2 * st]); v_fma(tt[2 * st + 1], a3[A & 1][n][2 * st + 1], __uint_as_float(rs[A]), kk[4 * n + 2 * st + 1]); }
        else if constexpr (st < 4) { v_relu(tt[2 * st - 4]); v_relu(tt[2 * st - 3]); }
        else if constexpr (st < 6) { v_sqr(tt[2 * st - 8]); v_sqr(tt[2 * st - 7]); }
        else if constexpr (n < 2) { v_cvt(gp[n][0], tt[0], tt[1]); v_cvt(gp[n][1], tt[2], tt[3]); }
        else { v_cvt(gp[2][0], tt[0], tt[1]); v_cvt(gp[3][0], tt[0], tt[1]); v_cvt(gp[2][1], tt[2], tt[3]); v_cvt(gp[3][1], tt[2], tt[3]); }
      } else if constexpr (e.kind == E_GFLAG) {
        wait_vm<FP.vm(FP.last(E_ACT, A, 22, I), I)>();
        put_flag(rws, vF + A * 4096, ep);
      } else if constexpr (e.kind == E_W4R) { ldW4(IC<A>{});
      } else if constexpr (e.kind == E_W4D) { ldW4(IC<NR4 + NL4 + NF4 + A>{});
      } else if constexpr (e.kind == E_W4L) {
        bulk(IC<60 - N3_X + A>{});
      } else if constexpr (e.kind == E_W4F) {
        constexpr int i = NR4 + NL4 + A;
        set_m0n<(A / 5) * 20480 + (i % 5) * 768>(mF);
        dma<(i % 5) * 256, 2>(vW, rws, s4[i / 5]);
      } else if constexpr (e.kind == E_UNP) {
        if constexpr (A == 0 && NL_PRE < NL4) wait_vm<FP.vm(FP.last(E_W4L, NL4 - 1, -1, I), I)>();
        if constexpr (A == NL4) wait_vm<FP.vm(FP.last(E_W4F, NF4 - 1, -1, I), I)>();
        if constexpr (A < NL4) lds_rd_a<A * 1024>(W4[NR4 + A], rdP); else lds_rd_a<((A - NL4) / 5) * 20480 + ((NR4 + A) % 5) * 1024>(W4[NR4 + A], rdF);
      } else if constexpr (e.kind == E_FLD) {
        if constexpr (A == 4) fl = __builtin_amdgcn_raw_buffer_load_b64(rws, i16 * 128, WS_FH + p * 4096 + 2048, 16);
        else fl = __builtin_amdgcn_raw_buffer_load_b64(rws, vC, WS_FG + (p * 128 + A * 32 + w * 8) * 128, 16);
        asm volatile("" ::: "memory");
      } else if constexpr (e.kind == E_CHK) {
        pin2(fl);
        if (__builtin_expect(flag_missing(fl, ep), 0)) {
          if constexpr (A == 4) poll(rws, WS_FH + p * 4096 + 2048, i16 * 128, ep);
          else poll(rws, WS_FG + (p * 128 + A * 32 + w * 8) * 128, vC, ep);
        }
        asm volatile("" ::: "memory");
      } else if constexpr (e.kind == E_AG) {
        if constexpr (Bv == 0) { vA[A] = l16; pinv(vA[A]); vA[A] += A * 49152; }
        ag[A][Bv] = ld_sc1(rws, vA[A], sG[Bv]); asm volatile("" ::: "memory");
      } else if constexpr (e.kind == E_INIT) {
        unsigned r = rot4; pins(r);
        sG[A] = WS_G8 + p * 196608 + (w * 12 + (A + r >= 12 ? A + r - 12 : A + r)) * 1024; pins(sG[A]);
      } else if constexpr (e.kind == E_RW) {
        if constexpr (A == 4) lds_wr_f4<2 * RED_B + Bv * 64>(wrR, y0[Bv]); else lds_wr_f4<(A & 1) * RED_B + Bv * 64>(wrR, acc[A & 1][Bv]);
      } else if constexpr (e.kind == E_BARR) {
        BARRIER();
      } else if constexpr (e.kind == E_RR) {
        lds_rd_f4<(Bv < 8 ? (A & 1) * RED_B + (Bv / 2) * RED_W : 2 * RED_B + A * RED_W) + (Bv & 1) * 16>(rr4[Bv], rdR);
      } else if constexpr (e.kind == E_RADD) {
        if constexpr (Bv < 16) {
          constexpr int lv = Bv / 4, c0 = (Bv % 4) * 2;
          if constexpr (lv == 0) { v_add2(o8[c0], rr4[c0 / 4][c0 % 4], rr4[2 + c0 / 4][c0 % 4]); v_add2(o8[c0 + 1], rr4[c0 / 4][c0 % 4 + 1], rr4[2 + c0 / 4][c0 % 4 + 1]); }
          else { v_add(o8[c0], rr4[2 * (lv + 1) + c0 / 4][c0 % 4]); v_add(o8[c0 + 1], rr4[2 * (lv + 1) + c0 / 4][c0 % 4 + 1]); }
        } else { v_cvt(op[2 * (Bv - 16)], o8[4 * (Bv - 16)], o8[4 * (Bv - 16) + 1]); v_cvt(op[2 * (Bv - 16) + 1], o8[4 * (Bv - 16) + 2], o8[4 * (Bv - 16) + 3]); }
      } else if constexpr (e.kind == E_OUT) {
        __builtin_amdgcn_raw_buffer_store_b128((u32x4){op[0], op[1], op[2], op[3]}, ro, vO + A * 20480, 0, A < 3 ? OPOL : 0); asm volatile("" ::: "memory");
      }
    };
    static_for<FP.first[0], FP.first[1]>(emit);
    static_for<0, 8>([&](auto bc) {
      static_for<0, 60>([&](auto gc) {
        constexpr int B = decltype(bc)::value, g = gc.value, G = B * 60 + g;
        if constexpr (B < 4) mfma_w<true, (g < 3)>(a3[B & 1][g % 3], W3[g], af[(G / 3) % 6]);
        else mfma_x<(g >= NV4), ((B & 1) == 1), (g < 5)>(acc[B & 1][g % 5], W4[g], ag[B - 4][g / 5]);
        static_for<FP.first[G + 1], FP.first[G + 2]>(emit);
      });
    });
  }
  asm volatile(".Lfnet_end:");
}

extern "C" void kernel_launch(void* const* d_in, const int* in_sizes, int n_in, void* d_out, int out_size, void* d_ws, size_t ws_size, hipStream_t stream) {
  (void)hipFuncSetAttribute((const void*)fnet_kernel, hipFuncAttributeMaxDynamicSharedMemorySize, L_END);
  fnet_kernel<<<256, 256, L_END, stream>>>((const u16*)d_in[0], (const u16*)d_in[1], (const u16*)d_in[2], (const u16*)d_in[3], (const u16*)d_in[4],
      (const u16*)d_in[6], (const u16*)d_in[7], (const u16*)d_in[8], (const u16*)d_in[9], (u16*)d_out, (char*)d_ws);
}
```
